```python
import torch

def _softmax(x, axis=-1):
    return torch.softmax(x, dim=axis)

def _gelu(x):
    return torch.nn.functional.gelu(x, approximate='tanh')

def _silu(x):
    return torch.nn.functional.silu(x)

def _sigmoid(x):
    return torch.sigmoid(x)

def _layernorm(x, axis=-1, eps=1e-05):
    m = x.mean(dim=axis, keepdim=True)
    v = x.var(dim=axis, keepdim=True, unbiased=False)
    return (x - m) / torch.sqrt(v + eps)

def _scatter_add(tgt, idx, src, axis=0):
    return tgt.clone().index_add_(axis, idx, src)

def _segment_sum(src, seg, n):
    out = torch.zeros((n,) + src.shape[1:], dtype=src.dtype)
    return out.index_add_(0, seg, src)

def _pad(x, pw):
    flat = tuple((v for pair in reversed(pw) for v in pair))
    return torch.nn.functional.pad(x, flat)
kernel_name = 'adaLN_cond__ssm_conv_h3__moe_arch_mod__prompt'

def setup_inputs():

    _w = lambda a: (lambda t: t.to(torch.bfloat16) if t.is_floating_point() else t)(torch.as_tensor(a))

    def _A_setup():
        B, S, D = (1, 2048, 640)
        ks = 4
        return {'q': _w((lambda *s: torch.randn(*s, dtype=torch.bfloat16) * 0.05)(B, S, D)), 'k': _w((lambda *s: torch.randn(*s, dtype=torch.bfloat16) * 0.4)(B, S, D)), 'v': _w((lambda *s: torch.randn(*s, dtype=torch.bfloat16) * 0.4)(B, S, D)), 'shift_k': _w((lambda *s: torch.randn(*s, dtype=torch.bfloat16) * 0.4)(D, ks) * 0.5), 'ssm_k': _w((lambda *s: torch.randn(*s, dtype=torch.bfloat16) * 0.8)(D, S) * (1.0 / S ** 0.5)), 'ssm_D': _w((lambda *s: torch.randn(*s, dtype=torch.bfloat16) * 0.4)(D))}
    _A_in = _A_setup()

    def _B_setup():
        B, S, D, F, C = (1, 2048, 640, 1728, 2)
        return {'x': _w((lambda *s: torch.randn(*s, dtype=torch.bfloat16) * 0.05)(B, S, D)), 'w_r': _w((lambda *s: torch.randn(*s, dtype=torch.bfloat16) * 0.05)(D)), 'w_gate': _w((lambda *s: torch.randn(*s, dtype=torch.bfloat16) * 0.05)(D, F)), 'w_up': _w((lambda *s: torch.randn(*s, dtype=torch.bfloat16) * 0.05)(D, F)), 'w_down': _w((lambda *s: torch.randn(*s, dtype=torch.bfloat16) * 0.05)(F, D))}
    _B_in = _B_setup()
    return {'A_q': _A_in['q'], 'A_k': _A_in['k'], 'A_v': _A_in['v'], 'A_shift_k': _A_in['shift_k'], 'A_ssm_k': _A_in['ssm_k'], 'A_ssm_D': _A_in['ssm_D'], 'B_x': _B_in['x'], 'B_w_r': _B_in['w_r'], 'B_w_gate': _B_in['w_gate'], 'B_w_up': _B_in['w_up'], 'B_w_down': _B_in['w_down'], 'c': _w((lambda *s: torch.randn(*s, dtype=torch.bfloat16) * 0.2)(1, 640) * 8.0), 'w_mod': _w((lambda *s: torch.randn(*s, dtype=torch.bfloat16) * 0.2)(640, 6 * 640) * 640 ** (-0.5) * 8.0)}

def reference(A_q, A_k, A_v, A_shift_k, A_ssm_k, A_ssm_D, B_x, B_w_r, B_w_gate, B_w_up, B_w_down, c, w_mod):

    def _A_ref(q, k, v, shift_k, ssm_k, ssm_D):
        B, S, D = q.shape
        ks = shift_k.shape[-1]
        k_pad = _pad(k, ((0, 0), (ks - 1, 0), (0, 0)))
        k_shift = torch.zeros_like(k)
        for t in range(ks):
            k_shift = k_shift + k_pad[:, t:t + S, :] * shift_k[:, ks - 1 - t][None, None, :]
        kv = k_shift * v
        L = 2 * S
        kv_pad = _pad(kv, ((0, 0), (0, S), (0, 0)))
        kf_pad = _pad(ssm_k, ((0, 0), (0, S)))
        kv_f = (lambda x, axes=(-2, -1): torch.fft.rfft2(x.float(), dim=axes))(kv_pad[:, None, :, :], axes=(1, 2))
        kf_f = (lambda x, axes=(-2, -1): torch.fft.rfft2(x.float(), dim=axes))(kf_pad[:, None, :], axes=(1, 2))
        kf_f = (lambda x, *d: x.permute(*d))(kf_f, 1, 2, 0)
        y_f = kv_f * kf_f[None, :, :, :]
        y = (lambda x, s, axes=(-2, -1): torch.fft.irfft2(x, s=s, dim=axes).to(torch.bfloat16))(y_f, (1, L), axes=(1, 2))[:, 0, :S, :]
        y = y + kv * ssm_D[None, None, :]
        return q * y

    def _B_ref(x, w_r, w_gate, w_up, w_down):
        B, S, D = x.shape
        C = 2
        r = x @ w_r
        idx = torch.argsort(-r, axis=-1)[:, :C]
        idx_e = (lambda x, n, axis: torch.repeat_interleave(x, n, dim=axis))(idx.reshape(B, C, 1), D, axis=2)
        x_sel = (lambda x, i, axis: torch.take_along_dim(x, i, dim=axis))(x, idx_e, axis=1)
        g = _silu(x_sel @ w_gate)
        u = x_sel @ w_up
        y = g * u @ w_down
        r_sel = _sigmoid((lambda x, i, axis: torch.take_along_dim(x, i, dim=axis))(r, idx, axis=1))
        y = y * r_sel.reshape(B, C, 1)
        b_off = (torch.arange(B) * S).reshape(B, 1)
        flat_idx = (idx + b_off).reshape(B * C)
        zero = (x * 0.0).reshape(B * S, D)
        upd = _scatter_add(zero, flat_idx, y.reshape(B * C, D), axis=0)
        return x + upd.reshape(B, S, D)
    x = A_q
    mod = (_silu(c) @ w_mod).reshape(c.shape[0], 6, 1, -1)
    sa, ca, ga, sb, cb, gb = (mod[:, 0].clone(), mod[:, 1].clone(), mod[:, 2].clone(), mod[:, 3].clone(), mod[:, 4].clone(), mod[:, 5].clone())
    h = _layernorm(x, axis=-1) * (1 + ca) + sa
    x = x + ga * _A_ref(h, A_k, A_v, A_shift_k, A_ssm_k, A_ssm_D)
    h = _layernorm(x, axis=-1) * (1 + cb) + sb
    x = x + gb * _B_ref(h, B_w_r, B_w_gate, B_w_up, B_w_down)
    return x
```

```cpp
#include <hip/hip_runtime.h>
#include <hip/hip_bf16.h>
#include <stdint.h>
typedef unsigned short u16;
#define S_ 2048
#define D_ 640
#define F_ 1728
__device__ __forceinline__ float bf2f(u16 x){ return __uint_as_float(((unsigned)x)<<16); }
__device__ __forceinline__ u16 f2bf(float f){ unsigned u=__float_as_uint(f); u += 0x7FFF + ((u>>16)&1); return (u16)(u>>16); }
__device__ __forceinline__ float rb(float f){ return bf2f(f2bf(f)); }

__global__ void k_mod(const u16* c, const u16* w, float* mod){
  int j = blockIdx.x*64 + (threadIdx.x&63); int part = threadIdx.x>>6;
  __shared__ float red[4][64];
  float acc=0;
  for(int i=part*160;i<part*160+160;i++){ float cv=bf2f(c[i]); float s=rb(cv/(1.f+__expf(-cv))); acc += s*bf2f(w[(size_t)i*3840+j]); }
  red[part][threadIdx.x&63]=acc; __syncthreads();
  if(part==0) mod[j]=rb(red[0][j&63]+red[1][j&63]+red[2][j&63]+red[3][j&63]);
}
__global__ void k_kv(const u16* k,const u16* v,const u16* sh,u16* kvT){
  int t=blockIdx.x;
  for(int d=threadIdx.x; d<D_; d+=blockDim.x){
    float a=0;
    for(int j=0;j<4;j++){ int tt=t-3+j; float kk = tt>=0? bf2f(k[tt*D_+d]):0.f; a = rb(a + rb(kk*bf2f(sh[d*4+3-j]))); }
    kvT[d*S_+t]=f2bf(a*bf2f(v[t*D_+d]));
  }
}
__global__ void k_conv(const u16* kvT,const u16* K,const u16* Dv,u16* y){
  int d=blockIdx.x; __shared__ float xs[S_], ks[S_];
  for(int i=threadIdx.x;i<S_;i+=blockDim.x){ xs[i]=bf2f(kvT[d*S_+i]); ks[i]=bf2f(K[d*S_+i]); }
  __syncthreads();
  float dd=bf2f(Dv[d]);
  for(int t=threadIdx.x;t<S_;t+=blockDim.x){ float a=0; for(int s=0;s<=t;s++) a+=ks[t-s]*xs[s]; y[t*D_+d]=f2bf(rb(a)+rb(xs[t]*dd)); }
}
__device__ float blk_sum(float v, float* red){ for(int o=32;o;o>>=1) v+=__shfl_xor(v,o); __syncthreads(); if((threadIdx.x&63)==0) red[threadIdx.x>>6]=v; __syncthreads(); float s=0; for(int i=0;i<blockDim.x/64;i++) s+=red[i]; return s; }
__global__ void k_tok(const u16* q,const u16* y,const float* mod,const u16* wr,u16* x1o,u16* h2o,float* r,u16* out){
  int t=blockIdx.x, d=threadIdx.x; __shared__ float red[16];
  float x=bf2f(q[t*D_+d]);
  float m=blk_sum(x,red)/D_; float vv=blk_sum((x-m)*(x-m),red)/D_;
  float mb=rb(m), vb=rb(vv);
  float n=rb(rb(x-mb)/rb(sqrtf(rb(vb+1e-5f))));
  float h=rb(rb(n*rb(1.f+mod[640+d]))+mod[d]);
  float a=rb(h*bf2f(y[t*D_+d]));
  float x1=rb(x+rb(mod[1280+d]*a));
  m=blk_sum(x1,red)/D_; vv=blk_sum((x1-m)*(x1-m),red)/D_;
  mb=rb(m); vb=rb(vv);
  n=rb(rb(x1-mb)/rb(sqrtf(rb(vb+1e-5f))));
  float h2=rb(rb(n*rb(1.f+mod[4*640+d]))+mod[3*640+d]);
  float rr=blk_sum(h2*bf2f(wr[d]),red);
  if(d==0) r[t]=rb(rr);
  x1o[t*D_+d]=f2bf(x1); h2o[t*D_+d]=f2bf(h2);
  out[t*D_+d]=f2bf(x1+rb(mod[5*640+d]*h2));
}
__global__ void k_top(const float* r,int* idx){
  if(threadIdx.x==0){ int i0=-1,i1=-1; float b0=-INFINITY,b1=-INFINITY;
    for(int t=0;t<S_;t++){ float v=r[t]; if(v>b0){b1=b0;i1=i0;b0=v;i0=t;} else if(v>b1){b1=v;i1=t;} }
    idx[0]=i0; idx[1]=i1; }
}
__global__ void k_gu(const u16* h2,const int* idx,const u16* wg,const u16* wu,float* a){
  int f=blockIdx.x*64+threadIdx.x; int c=blockIdx.y; const u16* x=h2+idx[c]*D_;
  float g=0,u=0; for(int d=0;d<D_;d++){ float xv=bf2f(x[d]); g+=xv*bf2f(wg[d*F_+f]); u+=xv*bf2f(wu[d*F_+f]); }
  g=rb(g); u=rb(u); g=rb(g/(1.f+__expf(-g))); a[c*F_+f]=rb(g*u);
}
__global__ void k_down(const float* a,const int* idx,const float* r,const u16* wd,const u16* x1,const u16* h2,const float* mod,u16* out){
  int d=blockIdx.x*64+threadIdx.x; int c=blockIdx.y; int t=idx[c];
  float s=0; for(int f=0;f<F_;f++) s+=a[c*F_+f]*bf2f(wd[f*D_+d]);
  s=rb(s); float rs=rb(1.f/(1.f+__expf(-r[t]))); s=rb(s*rs);
  float b=rb(bf2f(h2[t*D_+d])+s);
  out[t*D_+d]=f2bf(bf2f(x1[t*D_+d])+rb(mod[5*640+d]*b));
}
extern "C" void kernel_launch(void* const* d_in, const int* in_sizes, int n_in, void* d_out, int out_size, void* d_ws, size_t ws_size, hipStream_t stream){
  const u16* q=(const u16*)d_in[0]; const u16* k=(const u16*)d_in[1]; const u16* v=(const u16*)d_in[2];
  const u16* sh=(const u16*)d_in[3]; const u16* K=(const u16*)d_in[4]; const u16* Dv=(const u16*)d_in[5];
  const u16* wr=(const u16*)d_in[7]; const u16* wg=(const u16*)d_in[8]; const u16* wu=(const u16*)d_in[9]; const u16* wd=(const u16*)d_in[10];
  const u16* c=(const u16*)d_in[11]; const u16* wm=(const u16*)d_in[12];
  char* ws=(char*)d_ws; float* mod=(float*)ws; float* r=(float*)(ws+16384); int* idx=(int*)(ws+32768); float* a=(float*)(ws+40960);
  u16* kvT=(u16*)(ws+(1<<20)); u16* y=(u16*)(ws+(4<<20)); u16* x1=(u16*)(ws+(8<<20)); u16* h2=(u16*)(ws+(12<<20));
  u16* out=(u16*)d_out;
  k_mod<<<60,256,0,stream>>>(c,wm,mod);
  k_kv<<<S_,320,0,stream>>>(k,v,sh,kvT);
  k_conv<<<D_,1024,0,stream>>>(kvT,K,Dv,y);
  k_tok<<<S_,640,0,stream>>>(q,y,mod,wr,x1,h2,r,out);
  k_top<<<1,64,0,stream>>>(r,idx);
  k_gu<<<dim3(27,2),64,0,stream>>>(h2,idx,wg,wu,a);
  k_down<<<dim3(10,2),64,0,stream>>>(a,idx,r,wd,x1,h2,mod,out);
}
```

```cpp
#include <hip/hip_runtime.h>
#include <hip/hip_bf16.h>
#include <stdint.h>

typedef unsigned short u16;
typedef unsigned int u32;
typedef unsigned long long u64;
typedef u32 u32x2 __attribute__((ext_vector_type(2)));
typedef u32 u32x4 __attribute__((ext_vector_type(4)));
typedef float f32x2 __attribute__((ext_vector_type(2)));
typedef float f32x4 __attribute__((ext_vector_type(4)));
typedef short s16x4 __attribute__((ext_vector_type(4)));
typedef __bf16 bf16x2 __attribute__((ext_vector_type(2)));
typedef __bf16 bf16x8 __attribute__((ext_vector_type(8)));
#define LAS __attribute__((address_space(3)))
#define GAS __attribute__((address_space(1)))
#define RLX_AGENT __ATOMIC_RELAXED, __HIP_MEMORY_SCOPE_AGENT
#define DI __device__ __forceinline__

constexpr int S_ = 2048, D_ = 640, F_ = 1728;
constexpr u32 SENT = 0xFFFFFFFFu;
constexpr u32 SPIN_CAP = 1u << 16;
constexpr u32 OFF_E = 0, OFF_FLAG = 4096, OFF_MOD = 8192, OFF_SCORE = 15872, OFF_A = 17920;
constexpr u32 OFF_XT = 32768, OFF_YB = OFF_XT + 2621440, OFF_CAND = OFF_YB + 2621440, OFF_END = OFF_CAND + 1310720;
constexpr u32 OFF_DBG = 8u << 20;
constexpr u32 KC_STRIDE = 4160, XL = 66560, XL_STRIDE = 4640, RED1 = 86016, RED2 = RED1 + 640, RR = RED2 + 640,
              SYNCW = RR + 1024, WINW = SYNCW + 16, LDS_BYTES = WINW + 16;

#define STAMP(i) do {} while (0)

DI float lo16(u32 x) { return __uint_as_float(x << 16); }
DI float hi16(u32 x) { return __uint_as_float(x & 0xffff0000u); }
DI u32 pkbf(float lo, float hi) { f32x2 f = {lo, hi}; return __builtin_bit_cast(u32, __builtin_convertvector(f, bf16x2)); }
DI float rbf(float x) { return lo16(pkbf(x, x)); }
DI u32 san(u32 x) { return x < 0xFFFFFFFEu ? x : 0xFFFFFFFEu; }
DI u32 umax2(u32 a, u32 b) { return a > b ? a : b; }
DI u32 umax4(u32x4 v) { return umax2(umax2(v.x, v.y), umax2(v.z, v.w)); }
DI float sigm(float x) { return __builtin_amdgcn_rcpf(1.f + __expf(-x)); }
template <int CTRL> DI u32 dppu(u32 x) { return (u32)__builtin_amdgcn_update_dpp(0, (int)x, CTRL, 0xf, 0xf, false); }
template <int CTRL> DI float dppf(float x) { return __uint_as_float(dppu<CTRL>(__float_as_uint(x))); }
DI float swap32_add(float a, float b) { auto r = __builtin_amdgcn_permlane32_swap(__float_as_uint(a), __float_as_uint(b), false, false); return __uint_as_float(r[0]) + __uint_as_float(r[1]); }
DI float swap16_add(float a, float b) { auto r = __builtin_amdgcn_permlane16_swap(__float_as_uint(a), __float_as_uint(b), false, false); return __uint_as_float(r[0]) + __uint_as_float(r[1]); }
DI float row_sum(float x) { x += dppf<0x128>(x); x += dppf<0x124>(x); x += dppf<0x122>(x); x += dppf<0x121>(x); return x; }
DI u32 wave_umax(u32 x) {
  x = umax2(x, dppu<0x128>(x)); x = umax2(x, dppu<0x124>(x)); x = umax2(x, dppu<0x122>(x)); x = umax2(x, dppu<0x121>(x));
  auto r = __builtin_amdgcn_permlane16_swap(x, x, false, false); x = umax2(r[0], r[1]);
  auto s = __builtin_amdgcn_permlane32_swap(x, x, false, false); return umax2(s[0], s[1]);
}
DI f32x4 mfma16(u32x4 a, u32x4 b, f32x4 c) { return __builtin_amdgcn_mfma_f32_16x16x32_bf16(__builtin_bit_cast(bf16x8, a), __builtin_bit_cast(bf16x8, b), c, 0, 0, 0); }
#define LDS128(off) (*(LAS u32x4*)(smem + (off)))
#define LDSF4(off) (*(LAS f32x4*)(smem + (off)))
#define LDS32(off) (*(LAS u32*)(smem + (off)))
#define RFL(x) ((u32)__builtin_amdgcn_readfirstlane((int)(x)))
#define CFENCE() asm volatile("" ::: "memory")

DI void reduce24(const float (&V)[8][3], LAS char* smem, u32 dst, int lane) {
  float W[4][3], X[2][3];
#pragma unroll
  for (int t = 0; t < 4; ++t)
#pragma unroll
    for (int q = 0; q < 3; ++q) W[t][q] = swap32_add(V[t][q], V[t + 4][q]);
#pragma unroll
  for (int t = 0; t < 2; ++t)
#pragma unroll
    for (int q = 0; q < 3; ++q) X[t][q] = row_sum(swap16_add(W[t][q], W[t + 2][q]));
  const int sel = lane & 15;
  if (sel < 2) {
    f32x4 o = {sel ? X[1][0] : X[0][0], sel ? X[1][1] : X[0][1], sel ? X[1][2] : X[0][2], 0.f};
    LDSF4(dst + (sel + 2 * (lane >> 4)) * 16) = o;
  }
}
DI f32x4 gather5(LAS char* smem, u32 base, int lane) {
  f32x4 s = LDSF4(base + (lane & 7) * 16);
#pragma unroll
  for (int w = 1; w < 5; ++w) s += LDSF4(base + w * 128 + (lane & 7) * 16);
  return s;
}
DI float rdl(float x, int l) { return __uint_as_float((u32)__builtin_amdgcn_readlane((int)__float_as_uint(x), l)); }

template <int M>
DI void load_afrags(u32 addr, u32x4 (&A)[8][4]) {
  u32x2 o[16];
  asm volatile(
      "ds_read_b64 %0, %16 offset:%17\n\tds_read_b64 %1, %16 offset:%17+8\n\t"
      "ds_read_b64 %2, %16 offset:%17+64\n\tds_read_b64 %3, %16 offset:%17+72\n\t"
      "ds_read_b64 %4, %16 offset:%17+128\n\tds_read_b64 %5, %16 offset:%17+136\n\t"
      "ds_read_b64 %6, %16 offset:%17+192\n\tds_read_b64 %7, %16 offset:%17+200\n\t"
      "ds_read_b64 %8, %16 offset:%17+512\n\tds_read_b64 %9, %16 offset:%17+520\n\t"
      "ds_read_b64 %10, %16 offset:%17+576\n\tds_read_b64 %11, %16 offset:%17+584\n\t"
      "ds_read_b64 %12, %16 offset:%17+640\n\tds_read_b64 %13, %16 offset:%17+648\n\t"
      "ds_read_b64 %14, %16 offset:%17+704\n\tds_read_b64 %15, %16 offset:%17+712\n\t"
      "s_waitcnt lgkmcnt(0)"
      : "=&v"(o[0]), "=&v"(o[1]), "=&v"(o[2]), "=&v"(o[3]), "=&v"(o[4]), "=&v"(o[5]), "=&v"(o[6]), "=&v"(o[7]),
        "=&v"(o[8]), "=&v"(o[9]), "=&v"(o[10]), "=&v"(o[11]), "=&v"(o[12]), "=&v"(o[13]), "=&v"(o[14]), "=&v"(o[15])
      : "v"(addr), "i"(M * 512)
      : "memory");
#pragma unroll
  for (int r = 0; r < 4; ++r) {
    A[M][r] = u32x4{o[2 * r].x, o[2 * r].y, o[2 * r + 1].x, o[2 * r + 1].y};
    A[M + 1][r] = u32x4{o[8 + 2 * r].x, o[8 + 2 * r].y, o[8 + 2 * r + 1].x, o[8 + 2 * r + 1].y};
  }
}
DI u32x4 tr_frag(LAS char* p, u32 step) {
  typedef short v4i16_t __attribute__((ext_vector_type(4)));
  u32x2 a = __builtin_bit_cast(u32x2, __builtin_amdgcn_ds_read_tr16_b64_v4i16((LAS v4i16_t*)p));
  u32x2 b = __builtin_bit_cast(u32x2, __builtin_amdgcn_ds_read_tr16_b64_v4i16((LAS v4i16_t*)(p + step)));
  return u32x4{a.x, a.y, b.x, b.y};
}

__global__ void __launch_bounds__(512, 2)
fused_block(const u16* __restrict__ q, const u16* __restrict__ k, const u16* __restrict__ v,
            const u16* __restrict__ shiftk, const u16* __restrict__ ssmk, const u16* __restrict__ ssmD,
            const u16* __restrict__ wr, const u16* __restrict__ wg, const u16* __restrict__ wu,
            const u16* __restrict__ wd, const u16* __restrict__ cvec, const u16* __restrict__ wmod,
            u16* __restrict__ out, char* __restrict__ ws) {
  extern __shared__ __attribute__((aligned(16))) char smem_g[];
  LAS char* smem = (LAS char*)smem_g;
  const int tid = threadIdx.x, lane = tid & 63;
  const int wave = __builtin_amdgcn_readfirstlane(tid >> 6);
  const int b = blockIdx.x, xcd = b & 7, slot = b >> 3;
  const int j = xcd * 32 + slot;
  const bool is_conv = slot < 20;
  const int grp = xcd * 20 + slot;
  const int sa_ = slot - 20;
  const int gu_id = xcd * 7 + sa_, dn_id = xcd * 5 + sa_ - 7, md_id = xcd + 8 * (11 - sa_);
  const bool is_gu = !is_conv && sa_ < 7 && gu_id < 54;
  const bool is_dn = !is_conv && sa_ >= 7;
  const bool is_md = !is_conv && md_id < 60;
  const __amdgpu_buffer_rsrc_t rs = __builtin_amdgcn_make_buffer_rsrc((void*)ws, (short)0, (int)OFF_END, 0x00020000);
#define LD128(off) __builtin_amdgcn_raw_buffer_load_b128(rs, (off), 0, 16)
#define LD32(off) __builtin_amdgcn_raw_buffer_load_b32(rs, (off), 0, 16)
#define ST128(val, off) __builtin_amdgcn_raw_buffer_store_b128((val), rs, (off), 0, 16)
  const u32x4 SENT4 = {SENT, SENT, SENT, SENT};
  const int li = lane & 15, lg = lane >> 4;

  STAMP(0);
  u32 kk[11], vv[8];
  u32x4 shv;
  u32 Dp = 0, wrp = 0;
  u32x4 kw[8];
  u32x2 kx[8];
  u64 tag = 0;
  if (wave < 5) {
#pragma unroll
    for (int i = 0; i < 11; ++i) {
      int row = 8 * j - 3 + i;
      row = row < 0 ? 0 : row;
      kk[i] = *(const u32*)(k + row * D_ + 2 * tid);
    }
#pragma unroll
    for (int i = 0; i < 8; ++i) vv[i] = *(const u32*)(v + (8 * j + i) * D_ + 2 * tid);
    shv = *(const u32x4*)(shiftk + 8 * tid);
    Dp = *(const u32*)(ssmD + 2 * tid);
    wrp = *(const u32*)(wr + 2 * tid);
  } else if (wave < 7) {
    if (is_conv) {
#pragma unroll
      for (int i = 0; i < 8; ++i) {
        const int uu = (wave - 5) * 512 + i * 64 + lane, c = uu >> 8, a = uu & 255;
        const int e = (8 * (grp >> 1) + 2 * c + (grp & 1)) * S_ + 8 * a, e2 = e + 8;
        kw[i] = *(const u32x4*)(ssmk + e);
        kx[i] = *(const u32x2*)(ssmk + (e2 < D_ * S_ - 4 ? e2 : D_ * S_ - 4));
      }
    }
  } else {
    const u64 e0 = __hip_atomic_load((GAS u64*)(ws + OFF_E), RLX_AGENT);
#pragma unroll
    for (int i = 0; i < 20; ++i) ST128(SENT4, OFF_XT + b * 20480 + i * 1024 + lane * 16);
    if (b < 17) ST128(SENT4, OFF_MOD + b * 1024 + lane * 16);
    tag = e0 + 1;
  }
  if (tid == 0) LDS32(SYNCW) = 0;
  if (is_conv && tid < 120) LDS128(XL + (tid / 30) * XL_STRIDE + (256 + tid % 30) * 16) = u32x4{0, 0, 0, 0};
  __builtin_amdgcn_s_waitcnt(0xC07F);
  __builtin_amdgcn_s_barrier();
  CFENCE();

  u32 kvp[2][4];
  if (wave < 5) {
    if (j == 0) { kk[0] = 0; kk[1] = 0; kk[2] = 0; }
    const f32x2 s0 = {lo16(shv.x), lo16(shv.z)}, s1 = {hi16(shv.x), hi16(shv.z)}, s2 = {lo16(shv.y), lo16(shv.w)}, s3 = {hi16(shv.y), hi16(shv.w)};
    f32x2 kf[11], kv[8];
#pragma unroll
    for (int i = 0; i < 11; ++i) kf[i] = f32x2{lo16(kk[i]), hi16(kk[i])};
#pragma unroll
    for (int t = 0; t < 8; ++t) {
      f32x2 a = kf[t] * s3;
      a = kf[t + 1] * s2 + a;
      a = kf[t + 2] * s1 + a;
      a = kf[t + 3] * s0 + a;
      kv[t] = a * f32x2{lo16(vv[t]), hi16(vv[t])};
    }
#pragma unroll
    for (int d = 0; d < 4; ++d) {
      kvp[0][d] = san(pkbf(kv[7 - 2 * d].x, kv[6 - 2 * d].x));
      kvp[1][d] = san(pkbf(kv[7 - 2 * d].y, kv[6 - 2 * d].y));
    }
    STAMP(1);
    for (u32 spins = 0; spins < (SPIN_CAP << 4); ++spins) {
      if (__hip_atomic_load((LAS u32*)(smem + SYNCW), __ATOMIC_RELAXED, __HIP_MEMORY_SCOPE_WORKGROUP)) break;
    }
    CFENCE();
    const u32 xo = OFF_XT + (tid >> 2) * 32768 + j * 64 + (tid & 3) * 16;
    ST128((u32x4{kvp[0][0], kvp[0][1], kvp[0][2], kvp[0][3]}), xo);
    ST128((u32x4{kvp[1][0], kvp[1][1], kvp[1][2], kvp[1][3]}), xo + 16384);
    ST128(SENT4, OFF_CAND + j * 5120 + tid * 16);
    STAMP(2);
  } else if (wave < 7) {
    if (is_conv) {
#pragma unroll
      for (int i = 0; i < 8; ++i) {
        const int uu = (wave - 5) * 512 + i * 64 + lane, c = uu >> 8, a = uu & 255;
        const u32 w0 = kw[i].x, w1 = kw[i].y, w2 = kw[i].z, w3 = kw[i].w, w4 = kx[i].x, w5 = kx[i].y;
        const u32 a0 = __builtin_amdgcn_alignbyte(w1, w0, 2), a1 = __builtin_amdgcn_alignbyte(w2, w1, 2),
                  a2 = __builtin_amdgcn_alignbyte(w3, w2, 2), a3 = __builtin_amdgcn_alignbyte(w4, w3, 2),
                  a4 = __builtin_amdgcn_alignbyte(w5, w4, 2);
        const u32 base = c * 4 * KC_STRIDE + (a + 2) * 16;
        LDS128(base) = kw[i];
        LDS128(base + KC_STRIDE) = u32x4{a0, a1, a2, a3};
        LDS128(base + 2 * KC_STRIDE) = u32x4{w1, w2, w3, w4};
        LDS128(base + 3 * KC_STRIDE) = u32x4{a1, a2, a3, a4};
        if ((i & 3) == 0 && lane == 0) {
          const u32x4 z = {0, 0, 0, 0};
#pragma unroll
          for (int r = 0; r < 4; ++r) LDS128(base - 32 + r * KC_STRIDE) = z;
          LDS128(base - 16) = z;
          LDS128(base - 16 + KC_STRIDE) = u32x4{0, 0, 0, w0 << 16};
          LDS128(base - 16 + 2 * KC_STRIDE) = u32x4{0, 0, 0, w0};
          LDS128(base - 16 + 3 * KC_STRIDE) = u32x4{0, 0, w0 << 16, a0};
        }
      }
    }
    STAMP(1);
  } else {
    asm volatile("s_waitcnt vmcnt(0)" ::: "memory");
    STAMP(1);
    if (lane == 0) __hip_atomic_store((GAS u64*)(ws + OFF_FLAG) + b, tag, RLX_AGENT);
    const u32 tl = (u32)tag, th = (u32)(tag >> 32);
    for (u32 spins = 0; spins < SPIN_CAP; ++spins) {
      CFENCE();
      const u32x4 f0 = LD128(OFF_FLAG + lane * 32), f1 = LD128(OFF_FLAG + lane * 32 + 16);
      const u32 bad = (f0.x ^ tl) | (f0.y ^ th) | (f0.z ^ tl) | (f0.w ^ th) | (f1.x ^ tl) | (f1.y ^ th) | (f1.z ^ tl) | (f1.w ^ th);
      if (__all(bad == 0)) break;
      __builtin_amdgcn_s_sleep(1);
    }
    CFENCE();
    if (lane == 0) {
      __hip_atomic_store((LAS u32*)(smem + SYNCW), 1u, __ATOMIC_RELAXED, __HIP_MEMORY_SCOPE_WORKGROUP);
      if (b == 0) __hip_atomic_store((GAS u64*)(ws + OFF_E), tag, RLX_AGENT);
    }
    STAMP(2);
  }

  u32 qd[8];
  u32x4 Wf[10];
  if (is_conv) {
    const int c = wave & 3, hf = wave >> 2;
    __syncthreads();
    STAMP(3);
    u32x4 A[8][4];
    const u32 aaddr = (u32)(uintptr_t)smem + (c * 4 + ((li + 1) & 3)) * KC_STRIDE + 16 * lg + 8 * ((li + 1) >> 2) + 256 * hf;
    load_afrags<0>(aaddr, A); load_afrags<2>(aaddr, A); load_afrags<4>(aaddr, A); load_afrags<6>(aaddr, A);
    STAMP(4);
    {
      u32x4 x0, x1;
      const u32 xo = OFF_XT + grp * 16384 + (wave * 128 + lane) * 16;
      for (u32 spins = 0; spins < SPIN_CAP; ++spins) {
        CFENCE();
        x0 = LD128(xo); x1 = LD128(xo + 1024);
        if (__all(umax2(umax4(x0), umax4(x1)) != SENT)) break;
        __builtin_amdgcn_s_sleep(1);
      }
      CFENCE();
      const u32 xa = XL + (lane & 3) * XL_STRIDE + (255 - 32 * wave - (lane >> 2)) * 16;
      LDS128(xa) = x0;
      LDS128(xa - 256) = x1;
    }
    STAMP(5);
    __syncthreads();
    STAMP(6);
    if (wave < 5) {
#pragma unroll
      for (int t = 0; t < 8; ++t) qd[t] = *(const u32*)(q + (8 * j + t) * D_ + 2 * tid);
    }
    const u32 bbase = XL + c * XL_STRIDE + (30 + 16 * hf - 2 * li + lg) * 16;
    const u32 pw = hf * 32768 + (lg & 1) * 2048 + (c * 32 + ((2 * li + (lg >> 1) + 4 * c) & 31)) * 16;
    const u32 pr = (tid >> 7) * 4096 + ((tid & 3) * 32 + ((((tid >> 2) & 31) + 4 * (tid & 3)) & 31)) * 16;
    const u32 yo = OFF_YB + (tid >> 2) * 10240 + grp * 64 + (tid & 3) * 16;
    f32x4 acc[8];
#pragma unroll
    for (int n = 0; n < 8; ++n) acc[n] = f32x4{0.f, 0.f, 0.f, 0.f};
    u32x4 B[2][4];
#pragma unroll
    for (int r = 0; r < 4; ++r) B[0][r] = LDS128(bbase + 7 * 512 + 64 * r);
#pragma unroll
    for (int cc = 0; cc < 8; ++cc) {
      if (cc < 7) {
#pragma unroll
        for (int r = 0; r < 4; ++r) B[(cc + 1) & 1][r] = LDS128(bbase + (6 - cc) * 512 + 64 * r);
      }
#pragma unroll
      for (int n = cc; n < 8; ++n)
#pragma unroll
        for (int r = 0; r < 4; ++r) acc[n] = mfma16(A[n - cc][r], B[cc & 1][r], acc[n]);
      LDSF4(pw + cc * 4096) = acc[cc];
      if (cc == 4 || cc == 7) {
        __syncthreads();
        const u32 p0 = pr + (cc == 7 ? 16384 : 0);
        const f32x4 lo = LDSF4(p0) + LDSF4(p0 + 32768), hi = LDSF4(p0 + 2048) + LDSF4(p0 + 32768 + 2048);
        ST128((u32x4{san(pkbf(lo.x, lo.y)), san(pkbf(lo.z, lo.w)), san(pkbf(hi.x, hi.y)), san(pkbf(hi.z, hi.w))}),
              yo + (cc == 7 ? 128 * 10240 : 0));
      }
    }
    STAMP(7);
  } else {
    if (wave < 5) {
#pragma unroll
      for (int t = 0; t < 8; ++t) qd[t] = *(const u32*)(q + (8 * j + t) * D_ + 2 * tid);
    }
    if (is_md) {
      u32x4 wm[10];
      float sv[10];
      const int rg = tid >> 3;
#pragma unroll
      for (int i = 0; i < 10; ++i) wm[i] = *(const u32x4*)(wmod + (rg + 64 * i) * 3840 + md_id * 64 + (tid & 7) * 8);
#pragma unroll
      for (int i = 0; i < 10; ++i) { const float x = lo16(cvec[rg + 64 * i]); sv[i] = rbf(x * sigm(x)); }
      float a[8];
#pragma unroll
      for (int e = 0; e < 8; ++e) a[e] = 0.f;
#pragma unroll
      for (int i = 0; i < 10; ++i)
#pragma unroll
        for (int e = 0; e < 4; ++e) { a[2 * e] += sv[i] * lo16(wm[i][e]); a[2 * e + 1] += sv[i] * hi16(wm[i][e]); }
      float y2[2];
#pragma unroll
      for (int e = 0; e < 2; ++e) {
        const float y = swap16_add(swap32_add(a[e], a[e + 4]), swap32_add(a[e + 2], a[e + 6]));
        y2[e] = y + dppf<0x128>(y);
      }
      if ((lane & 8) == 0) *(LAS f32x2*)(smem + wave * 256 + (lane & 7) * 32 + lg * 8) = f32x2{y2[0], y2[1]};
      __syncthreads();
      if (wave == 0) {
        f32x2 s = {0.f, 0.f};
#pragma unroll
        for (int w = 0; w < 8; ++w) s += *(LAS f32x2*)(smem + w * 256 + (lane & 31) * 8);
        const u32 p = san(pkbf(s.x, s.y));
        const u32x4 o = {p, dppu<0x55>(p), dppu<0xAA>(p), dppu<0xFF>(p)};
        if (lane < 32 && (lane & 3) == 0) ST128(o, OFF_MOD + md_id * 128 + lane * 4);
      }
      __syncthreads();
    }
    STAMP(3);
    if (is_gu) {
      u32x4 w[10];
      const u16* wsrc = ((tid & 4) ? wu : wg) + gu_id * 32 + (tid & 3) * 8;
#pragma unroll
      for (int i = 0; i < 10; ++i) w[i] = *(const u32x4*)(wsrc + ((tid >> 3) + 64 * i) * F_);
#pragma unroll
      for (int i = 0; i < 10; ++i) LDS128(((tid >> 2) & 1) * 40960 + ((tid >> 3) + 64 * i) * 64 + (tid & 3) * 16) = w[i];
      __syncthreads();
      LAS char* p = smem + ((wave >> 1) & 1) * 40960 + (320 * (wave >> 2) + 8 * lg + (li >> 2)) * 64 + (wave & 1) * 32 + (li & 3) * 8;
#pragma unroll
      for (int s = 0; s < 10; ++s) Wf[s] = tr_frag(p + s * 2048, 256);
    } else if (is_dn) {
      u32x4 w[7];
#pragma unroll
      for (int i = 0; i < 7; ++i) {
        int it = tid + 512 * i;
        it = it < 3456 ? it : 3455;
        w[i] = *(const u32x4*)(wd + (it >> 1) * D_ + dn_id * 16 + (it & 1) * 8);
      }
#pragma unroll
      for (int i = 0; i < 7; ++i) LDS128((tid + 512 * i) * 16) = w[i];
      __syncthreads();
      LAS char* p = smem + (32 * wave + 8 * lg + (li >> 2)) * 32 + (li & 3) * 8;
#pragma unroll
      for (int s = 0; s < 7; ++s) Wf[s] = tr_frag(p + s * 8192, 128);
      if (wave >= 6) Wf[6] = u32x4{0, 0, 0, 0};
    }
    STAMP(7);
  }

  f32x2 qf[8], ca1, sa, ga, cb1, sb, gb, gw;
  if (wave < 5) {
    u32 mp[6];
    for (u32 spins = 0; spins < SPIN_CAP; ++spins) {
      CFENCE();
#pragma unroll
      for (int i = 0; i < 6; ++i) mp[i] = LD32(OFF_MOD + i * 1280 + tid * 4);
      if (__all(umax2(umax2(umax2(mp[0], mp[1]), umax2(mp[2], mp[3])), umax2(mp[4], mp[5])) != SENT)) break;
      __builtin_amdgcn_s_sleep(2);
    }
    CFENCE();
    sa = f32x2{lo16(mp[0]), hi16(mp[0])};
    { const u32 t = pkbf(1.f + lo16(mp[1]), 1.f + hi16(mp[1])); ca1 = f32x2{lo16(t), hi16(t)}; }
    ga = f32x2{lo16(mp[2]), hi16(mp[2])};
    sb = f32x2{lo16(mp[3]), hi16(mp[3])};
    { const u32 t = pkbf(1.f + lo16(mp[4]), 1.f + hi16(mp[4])); cb1 = f32x2{lo16(t), hi16(t)}; }
    gb = f32x2{lo16(mp[5]), hi16(mp[5])};
    const f32x2 wrf = {lo16(wrp), hi16(wrp)};
    gw = cb1 * wrf;
    float V[8][3];
#pragma unroll
    for (int t = 0; t < 8; ++t) {
      qf[t] = f32x2{lo16(qd[t]), hi16(qd[t])};
      V[t][0] = qf[t].x + qf[t].y;
      V[t][1] = qf[t].x * qf[t].x + qf[t].y * qf[t].y;
      V[t][2] = 0.f;
    }
    V[0][2] = gw.x + gw.y;
    V[1][2] = sb.x * wrf.x + sb.y * wrf.y;
    reduce24(V, smem, RED1 + wave * 128, lane);
  }
  STAMP(8);
  __syncthreads();
  f32x2 x1[8];
  float Gs = 0.f, c0s = 0.f;
  if (wave < 6) {
    const f32x4 s = gather5(smem, RED1, lane);
    Gs = rdl(s.z, 0); c0s = rdl(s.z, 1);
    if (wave < 5) {
      const float m = s.x * (1.f / D_);
      const float rstd = __builtin_amdgcn_rsqf(fmaxf(s.y * (1.f / D_) - m * m, 0.f) + 1e-5f);
      const f32x2 Df = {lo16(Dp), hi16(Dp)};
      f32x2 hg[8], kd[8];
#pragma unroll
      for (int t = 0; t < 8; ++t) {
        const float mt = rdl(m, t), rt = rdl(rstd, t);
        hg[t] = ((qf[t] - mt) * rt * ca1 + sa) * ga;
        const u32 u0 = kvp[0][(7 - t) >> 1], u1 = kvp[1][(7 - t) >> 1];
        kd[t] = ((t & 1) ? f32x2{lo16(u0), lo16(u1)} : f32x2{hi16(u0), hi16(u1)}) * Df;
      }
      STAMP(9);
      u32x4 y0, y1;
      const u32 yo = OFF_YB + j * 10240 + (tid >> 2) * 128 + (tid & 3) * 16;
      for (u32 spins = 0; spins < SPIN_CAP; ++spins) {
        CFENCE();
        y0 = LD128(yo); y1 = LD128(yo + 64);
        if (__all(umax2(umax4(y0), umax4(y1)) != SENT)) break;
        __builtin_amdgcn_s_sleep(2);
      }
      CFENCE();
      STAMP(10);
      float V[8][3];
#pragma unroll
      for (int t = 0; t < 8; ++t) {
        const u32 u0 = y0[t >> 1], u1 = y1[t >> 1];
        const f32x2 yy = ((t & 1) ? f32x2{hi16(u0), hi16(u1)} : f32x2{lo16(u0), lo16(u1)}) + kd[t];
        x1[t] = hg[t] * yy + qf[t];
        V[t][0] = x1[t].x + x1[t].y;
        V[t][1] = x1[t].x * x1[t].x + x1[t].y * x1[t].y;
        V[t][2] = x1[t].x * gw.x + x1[t].y * gw.y;
      }
      reduce24(V, smem, RED2 + wave * 128, lane);
    }
  }
  __syncthreads();
  STAMP(11);
  u32 op[8];
  int tA = 0, tB = 0;
  if (wave < 6) {
    const f32x4 s = gather5(smem, RED2, lane);
    const float m = s.x * (1.f / D_);
    const float rstd = __builtin_amdgcn_rsqf(fmaxf(s.y * (1.f / D_) - m * m, 0.f) + 1e-5f);
    const float r = rstd * (s.z - m * Gs) + c0s;
    u32 ob = __float_as_uint(r);
    ob ^= (u32)((int)ob >> 31) | 0x80000000u;
    ob = ob < 0xFFFFE000u ? ob : 0xFFFFE000u;
    const u32 key = (ob & 0xFFFFF000u) | ((2047u - (8 * j + (lane & 7))) << 1);
    u32 mx = umax2(key, dppu<0x124>(key)); mx = umax2(mx, dppu<0x122>(mx)); mx = umax2(mx, dppu<0x121>(mx));
    u32 k2 = key == mx ? 0u : key;
    k2 = umax2(k2, dppu<0x124>(k2)); k2 = umax2(k2, dppu<0x122>(k2)); k2 = umax2(k2, dppu<0x121>(k2));
    const u32 kA = RFL(mx), kB = RFL(k2) | 1u;
    tA = (2047 - ((kA >> 1) & 2047)) & 7; tB = (2047 - ((kB >> 1) & 2047)) & 7;
    if (wave == 5) {
      if (lane == 0) __hip_atomic_store((GAS u64*)(ws + OFF_SCORE) + j, (u64)kA | ((u64)kB << 32), RLX_AGENT);
      STAMP(12);
      u32x4 g0, g1;
      for (u32 spins = 0; spins < SPIN_CAP; ++spins) {
        CFENCE();
        g0 = LD128(OFF_SCORE + lane * 32); g1 = LD128(OFF_SCORE + lane * 32 + 16);
        if (__all(umax2(umax4(g0), umax4(g1)) != SENT)) break;
        __builtin_amdgcn_s_sleep(1);
      }
      CFENCE();
      const u32 w0 = RFL(wave_umax(umax2(umax2(g0.x, g0.z), umax2(g1.x, g1.z))));
      u32 c2 = 0;
#pragma unroll
      for (int e = 0; e < 4; ++e) { c2 = umax2(c2, g0[e] == w0 ? 0u : g0[e]); c2 = umax2(c2, g1[e] == w0 ? 0u : g1[e]); }
      const u32 w1 = RFL(wave_umax(c2));
      if (lane == 0) *(LAS u32x2*)(smem + WINW) = u32x2{w0, w1};
      STAMP(13);
    } else {
      u32 d0 = 0, d1 = 0, d2 = 0, d3 = 0;
#pragma unroll
      for (int t = 0; t < 8; ++t) {
        const float mt = rdl(m, t), rt = rdl(rstd, t);
        const f32x2 h2 = (x1[t] - mt) * rt * cb1 + sb;
        const f32x2 o = gb * h2 + x1[t];
        op[t] = pkbf(o.x, o.y);
        const u32 hp = pkbf(h2.x, h2.y), xp = pkbf(x1[t].x, x1[t].y);
        d0 = t == tA ? hp : d0; d1 = t == tB ? hp : d1; d2 = t == tA ? xp : d2; d3 = t == tB ? xp : d3;
      }
      const bool o1 = lane & 1, o2 = lane & 2;
      u32 r = dppu<0xB1>(o1 ? d0 : d1); const u32 e0 = o1 ? r : d0, e1 = o1 ? d1 : r;
      r = dppu<0xB1>(o1 ? d2 : d3); const u32 e2 = o1 ? r : d2, e3 = o1 ? d3 : r;
      r = dppu<0x4E>(o2 ? e0 : e2); const u32 f0 = o2 ? r : e0, f2 = o2 ? e2 : r;
      r = dppu<0x4E>(o2 ? e1 : e3); const u32 f1 = o2 ? r : e1, f3 = o2 ? e3 : r;
      ST128((u32x4{san(f0), san(f1), san(f2), san(f3)}), OFF_CAND + j * 5120 + (tid & 3) * 1280 + (tid >> 2) * 16);
#pragma unroll
      for (int t = 0; t < 8; ++t)
        if (t != tA && t != tB) *(u32*)(out + (8 * j + t) * D_ + 2 * tid) = op[t];
      STAMP(12);
    }
  }
  __syncthreads();
  const u32x2 win = *(LAS u32x2*)(smem + WINW);
  const u32 w0 = RFL(win.x), w1 = RFL(win.y);
  const int tok0 = 2047 - ((w0 >> 1) & 2047), tok1 = 2047 - ((w1 >> 1) & 2047);
  if (wave < 5) {
#pragma unroll
    for (int t = 0; t < 8; ++t)
      if ((t == tA || t == tB) && 8 * j + t != tok0 && 8 * j + t != tok1) *(u32*)(out + (8 * j + t) * D_ + 2 * tid) = op[t];
  }
  const u32 row0 = OFF_CAND + (tok0 >> 3) * 5120 + (w0 & 1) * 1280, row1 = OFF_CAND + (tok1 >> 3) * 5120 + (w1 & 1) * 1280;
  if (is_gu) {
    u32x4 Bf[10];
    const u32 ro = (li ? row1 : row0) + (wave >> 2) * 640 + lg * 16;
    for (u32 spins = 0; spins < SPIN_CAP; ++spins) {
      CFENCE();
      u32 mx = 0;
#pragma unroll
      for (int s = 0; s < 10; ++s) Bf[s] = u32x4{0, 0, 0, 0};
      if (li < 2) {
#pragma unroll
        for (int s = 0; s < 10; ++s) Bf[s] = LD128(ro + s * 64);
#pragma unroll
        for (int s = 0; s < 10; ++s) mx = umax2(mx, umax4(Bf[s]));
      }
      if (__all(mx != SENT)) break;
      __builtin_amdgcn_s_sleep(1);
    }
    CFENCE();
    STAMP(14);
    f32x4 a0 = {0.f, 0.f, 0.f, 0.f}, a1 = a0;
#pragma unroll
    for (int s = 0; s < 10; s += 2) { a0 = mfma16(Wf[s], Bf[s], a0); a1 = mfma16(Wf[s + 1], Bf[s + 1], a1); }
    if (li < 2) LDSF4(RR + ((((wave >> 2) * 2 + ((wave >> 1) & 1)) * 2 + li) * 32 + (wave & 1) * 16 + 4 * lg) * 4) = a0 + a1;
    __syncthreads();
    if (wave == 0) {
      const u32 o = RR + (lane & 31) * 8;
      const f32x2 gg = *(LAS f32x2*)(smem + o) + *(LAS f32x2*)(smem + o + 512);
      const f32x2 uu = *(LAS f32x2*)(smem + o + 256) + *(LAS f32x2*)(smem + o + 768);
      const float g0 = rbf(gg.x), g1 = rbf(gg.y);
      const u32 p = san(pkbf(rbf(g0 * sigm(g0)) * rbf(uu.x), rbf(g1 * sigm(g1)) * rbf(uu.y)));
      const u32x4 ov = {p, dppu<0x55>(p), dppu<0xAA>(p), dppu<0xFF>(p)};
      if (lane < 32 && (lane & 3) == 0) ST128(ov, OFF_A + lg * 3456 + gu_id * 64 + li * 4);
    }
    STAMP(15);
  } else if (is_dn) {
    u32 hh = 0, xx = 0, gg = 0;
    if (wave == 0) {
      const u32 co = (dn_id * 16 + li) * 2 & ~3u, ro = ((lg & 1) ? row1 : row0) + co;
      for (u32 spins = 0; spins < SPIN_CAP; ++spins) {
        CFENCE();
        hh = LD32(ro); xx = LD32(ro + 2560); gg = LD32(OFF_MOD + 5 * 1280 + co);
        if (__all(umax2(hh, xx) != SENT)) break;
        __builtin_amdgcn_s_sleep(1);
      }
      CFENCE();
    }
    u32x4 Bf[7];
    const u32 ao = OFF_A + li * 3456 + wave * 64 + lg * 16;
    for (u32 spins = 0; spins < SPIN_CAP; ++spins) {
      CFENCE();
      u32 mx = 0;
#pragma unroll
      for (int s = 0; s < 7; ++s) Bf[s] = u32x4{0, 0, 0, 0};
      if (li < 2) {
#pragma unroll
        for (int s = 0; s < 6; ++s) Bf[s] = LD128(ao + s * 512);
        if (wave < 6) Bf[6] = LD128(ao + 6 * 512);
#pragma unroll
        for (int s = 0; s < 7; ++s) mx = umax2(mx, umax4(Bf[s]));
      }
      if (__all(mx != SENT)) break;
      __builtin_amdgcn_s_sleep(1);
    }
    CFENCE();
    STAMP(14);
    f32x4 a0 = {0.f, 0.f, 0.f, 0.f}, a1 = a0;
#pragma unroll
    for (int s = 0; s < 6; s += 2) { a0 = mfma16(Wf[s], Bf[s], a0); a1 = mfma16(Wf[s + 1], Bf[s + 1], a1); }
    a0 = mfma16(Wf[6], Bf[6], a0);
    if (li < 2) LDSF4(RR + ((wave * 2 + li) * 16 + 4 * lg) * 4) = a0 + a1;
    __syncthreads();
    if (wave == 0) {
      float s = 0.f;
#pragma unroll
      for (int w = 0; w < 8; ++w) s += *(LAS float*)(smem + RR + w * 128 + (lane & 31) * 4);
      const u32 kb = ((lg & 1) ? w1 : w0) & 0xFFFFF000u;
      const float rv = rbf(__uint_as_float((kb & 0x80000000u) ? (kb ^ 0x80000000u) : ~kb));
      const float y = rbf(rbf(s) * rbf(sigm(rv)));
      const bool hi = li & 1;
      const float h2 = hi ? hi16(hh) : lo16(hh), xv = hi ? hi16(xx) : lo16(xx), gv = hi ? hi16(gg) : lo16(gg);
      const float o = xv + rbf(gv * rbf(h2 + y));
      if (lane < 32) out[((lg & 1) ? tok1 : tok0) * D_ + dn_id * 16 + li] = (u16)pkbf(o, o);
    }
    STAMP(15);
  }
}

extern "C" void kernel_launch(void* const* d_in, const int* in_sizes, int n_in, void* d_out, int out_size,
                              void* d_ws, size_t ws_size, hipStream_t stream) {
  (void)in_sizes; (void)n_in; (void)out_size; (void)ws_size;
  (void)hipFuncSetAttribute((const void*)fused_block, hipFuncAttributeMaxDynamicSharedMemorySize, (int)LDS_BYTES);
  fused_block<<<256, 512, LDS_BYTES, stream>>>(
      (const u16*)d_in[0], (const u16*)d_in[1], (const u16*)d_in[2], (const u16*)d_in[3], (const u16*)d_in[4],
      (const u16*)d_in[5], (const u16*)d_in[7], (const u16*)d_in[8], (const u16*)d_in[9], (const u16*)d_in[10],
      (const u16*)d_in[11], (const u16*)d_in[12], (u16*)d_out, (char*)d_ws);
}
```

```cpp
#include <hip/hip_runtime.h>
#include <hip/hip_bf16.h>
#include <stdint.h>

typedef unsigned short u16;
typedef unsigned int u32;
typedef unsigned long long u64;
typedef u32 u32x2 __attribute__((ext_vector_type(2)));
typedef u32 u32x4 __attribute__((ext_vector_type(4)));
typedef float f32x2 __attribute__((ext_vector_type(2)));
typedef float f32x4 __attribute__((ext_vector_type(4)));
typedef short s16x4 __attribute__((ext_vector_type(4)));
typedef __bf16 bf16x2 __attribute__((ext_vector_type(2)));
typedef __bf16 bf16x8 __attribute__((ext_vector_type(8)));
#define LAS __attribute__((address_space(3)))
#define GAS __attribute__((address_space(1)))
#define RLX_AGENT __ATOMIC_RELAXED, __HIP_MEMORY_SCOPE_AGENT
#define DI __device__ __forceinline__

constexpr int S_ = 2048, D_ = 640, F_ = 1728;
constexpr u32 SENT = 0xFFFFFFFFu;
constexpr u32 SPIN_CAP = 1u << 16;
constexpr u32 OFF_E = 0, OFF_FLAG = 4096, OFF_F5 = OFF_FLAG + 5 * 4096, OFF_F6 = OFF_F5 + 4096, OFF_SCORE = OFF_F6 + 5 * 4096,
              OFF_MOD = OFF_SCORE + 4096, OFF_A = OFF_MOD + 8192, OFF_RD = OFF_A + 8192, OFF_P = OFF_RD + 16384;
constexpr u32 OFF_XT = 131072, OFF_YB = OFF_XT + 2621440, OFF_CAND = OFF_YB + 2621440, OFF_END = OFF_CAND + 655360;
constexpr u32 OFF_DBG = 8u << 20;
constexpr u32 KC_STRIDE = 4160, XL = 66560, XL_STRIDE = 4640, UPB = 40960 + 32, RED1 = 86016, RED2 = RED1 + 640, RR = RED2 + 640,
              TAGW = RR + 1024, WINW = TAGW + 16, SIL = WINW + 16, HR = SIL + 2688, HR_STRIDE = 1344, LDS_BYTES = HR + 2 * HR_STRIDE;
#define DLY_Y 10
#define DLY_SC 16
#define DLY_A 16
#define DLY_W 16
#define DLY_C 24
#define DLY_X 0
#define OUT_AUX 16

#define STAMP(i) do {} while (0)

DI float lo16(u32 x) { return __uint_as_float(x << 16); }
DI float hi16(u32 x) { return __uint_as_float(x & 0xffff0000u); }
DI u32 pkbf(float lo, float hi) { f32x2 f = {lo, hi}; return __builtin_bit_cast(u32, __builtin_convertvector(f, bf16x2)); }
DI float rbf(float x) { return lo16(pkbf(x, x)); }
DI u32 san(u32 x) { return x < 0xFFFFFFFEu ? x : 0xFFFFFFFEu; }
DI u32 umax2(u32 a, u32 b) { return a > b ? a : b; }
DI u32 umax4(u32x4 v) { return umax2(umax2(v.x, v.y), umax2(v.z, v.w)); }
DI float sigm(float x) { return __builtin_amdgcn_rcpf(1.f + __expf(-x)); }
template <int CTRL> DI u32 dppu(u32 x) { return (u32)__builtin_amdgcn_update_dpp(0, (int)x, CTRL, 0xf, 0xf, false); }
template <int CTRL> DI float dppf(float x) { return __uint_as_float(dppu<CTRL>(__float_as_uint(x))); }
DI float swap32_add(float a, float b) { auto r = __builtin_amdgcn_permlane32_swap(__float_as_uint(a), __float_as_uint(b), false, false); return __uint_as_float(r[0]) + __uint_as_float(r[1]); }
DI float swap16_add(float a, float b) { auto r = __builtin_amdgcn_permlane16_swap(__float_as_uint(a), __float_as_uint(b), false, false); return __uint_as_float(r[0]) + __uint_as_float(r[1]); }
DI float row_sum(float x) { x += dppf<0x128>(x); x += dppf<0x124>(x); x += dppf<0x122>(x); x += dppf<0x121>(x); return x; }
DI u32 wave_umax(u32 x) {
  x = umax2(x, dppu<0x128>(x)); x = umax2(x, dppu<0x124>(x)); x = umax2(x, dppu<0x122>(x)); x = umax2(x, dppu<0x121>(x));
  auto r = __builtin_amdgcn_permlane16_swap(x, x, false, false); x = umax2(r[0], r[1]);
  auto s = __builtin_amdgcn_permlane32_swap(x, x, false, false); return umax2(s[0], s[1]);
}
DI f32x4 mfma16(u32x4 a, u32x4 b, f32x4 c) { return __builtin_amdgcn_mfma_f32_16x16x32_bf16(__builtin_bit_cast(bf16x8, a), __builtin_bit_cast(bf16x8, b), c, 0, 0, 0); }
#define LDS128(off) (*(LAS u32x4*)(smem + (off)))
#define LDSF4(off) (*(LAS f32x4*)(smem + (off)))
#define LDS32(off) (*(LAS u32*)(smem + (off)))
#define RFL(x) ((u32)__builtin_amdgcn_readfirstlane((int)(x)))
#define CFENCE() asm volatile("" ::: "memory")

#define RS_STEP(n) "v_add_f32_dpp %0, %0, %0 row_ror:" #n " row_mask:0xf bank_mask:0xf\n\tv_add_f32_dpp %1, %1, %1 row_ror:" #n " row_mask:0xf bank_mask:0xf\n\t" \
                   "v_add_f32_dpp %2, %2, %2 row_ror:" #n " row_mask:0xf bank_mask:0xf\n\tv_add_f32_dpp %3, %3, %3 row_ror:" #n " row_mask:0xf bank_mask:0xf\n\t"
#define RS_STEP3(n) "v_add_f32_dpp %4, %4, %4 row_ror:" #n " row_mask:0xf bank_mask:0xf\n\tv_add_f32_dpp %5, %5, %5 row_ror:" #n " row_mask:0xf bank_mask:0xf\n\t" \
                    "v_add_f32_dpp %6, %6, %6 row_ror:" #n " row_mask:0xf bank_mask:0xf\n\t"
template <int NQ>
DI void reduce_tok(const float (&V)[8][NQ], float ex, LAS char* smem, u32 dst, int lane) {
  float W[4][NQ], X[2][NQ];
#pragma unroll
  for (int t = 0; t < 4; ++t)
#pragma unroll
    for (int q = 0; q < NQ; ++q) W[t][q] = swap32_add(V[t][q], V[t + 4][q]);
#pragma unroll
  for (int t = 0; t < 2; ++t)
#pragma unroll
    for (int q = 0; q < NQ; ++q) X[t][q] = swap16_add(W[t][q], W[t + 2][q]);
  const int sel = lane & 15;
  const u32 ad = dst + (sel + 2 * (lane >> 4)) * 16;
  if constexpr (NQ == 2) {
    asm volatile("s_nop 1\n\t" RS_STEP(8) RS_STEP(4) RS_STEP(2) RS_STEP(1) "s_nop 0"
                 : "+v"(X[0][0]), "+v"(X[0][1]), "+v"(X[1][0]), "+v"(X[1][1]));
    if (sel < 2) *(LAS f32x2*)(smem + ad) = f32x2{sel ? X[1][0] : X[0][0], sel ? X[1][1] : X[0][1]};
  } else {
    asm volatile("s_nop 1\n\t" RS_STEP(8) RS_STEP3(8) RS_STEP(4) RS_STEP3(4) RS_STEP(2) RS_STEP3(2) RS_STEP(1) RS_STEP3(1) "s_nop 0"
                 : "+v"(X[0][0]), "+v"(X[0][1]), "+v"(X[1][0]), "+v"(X[1][1]), "+v"(X[0][2]), "+v"(X[1][2]), "+v"(ex));
    if (sel < 2) LDSF4(ad) = f32x4{sel ? X[1][0] : X[0][0], sel ? X[1][1] : X[0][1], sel ? X[1][2] : X[0][2], ex};
  }
}
DI f32x4 gather5(LAS char* smem, u32 base, int lane) {
  f32x4 s = LDSF4(base + (lane & 7) * 16);
#pragma unroll
  for (int w = 1; w < 5; ++w) s += LDSF4(base + w * 128 + (lane & 7) * 16);
  return s;
}
DI float rdl(float x, int l) { return __uint_as_float((u32)__builtin_amdgcn_readlane((int)__float_as_uint(x), l)); }

template <int M>
DI void load_afrags(u32 addr, u32x4 (&A)[8][4]) {
  u32x2 o[16];
  asm volatile(
      "ds_read_b64 %0, %16 offset:%17\n\tds_read_b64 %1, %16 offset:%17+8\n\t"
      "ds_read_b64 %2, %16 offset:%17+64\n\tds_read_b64 %3, %16 offset:%17+72\n\t"
      "ds_read_b64 %4, %16 offset:%17+128\n\tds_read_b64 %5, %16 offset:%17+136\n\t"
      "ds_read_b64 %6, %16 offset:%17+192\n\tds_read_b64 %7, %16 offset:%17+200\n\t"
      "ds_read_b64 %8, %16 offset:%17+512\n\tds_read_b64 %9, %16 offset:%17+520\n\t"
      "ds_read_b64 %10, %16 offset:%17+576\n\tds_read_b64 %11, %16 offset:%17+584\n\t"
      "ds_read_b64 %12, %16 offset:%17+640\n\tds_read_b64 %13, %16 offset:%17+648\n\t"
      "ds_read_b64 %14, %16 offset:%17+704\n\tds_read_b64 %15, %16 offset:%17+712\n\t"
      "s_waitcnt lgkmcnt(0)"
      : "=&v"(o[0]), "=&v"(o[1]), "=&v"(o[2]), "=&v"(o[3]), "=&v"(o[4]), "=&v"(o[5]), "=&v"(o[6]), "=&v"(o[7]),
        "=&v"(o[8]), "=&v"(o[9]), "=&v"(o[10]), "=&v"(o[11]), "=&v"(o[12]), "=&v"(o[13]), "=&v"(o[14]), "=&v"(o[15])
      : "v"(addr), "i"(M * 512)
      : "memory");
#pragma unroll
  for (int r = 0; r < 4; ++r) {
    A[M][r] = u32x4{o[2 * r].x, o[2 * r].y, o[2 * r + 1].x, o[2 * r + 1].y};
    A[M + 1][r] = u32x4{o[8 + 2 * r].x, o[8 + 2 * r].y, o[8 + 2 * r + 1].x, o[8 + 2 * r + 1].y};
  }
}
DI u32x4 tr_frag(LAS char* p, u32 step) {
  typedef short v4i16_t __attribute__((ext_vector_type(4)));
  u32x2 a = __builtin_bit_cast(u32x2, __builtin_amdgcn_ds_read_tr16_b64_v4i16((LAS v4i16_t*)p));
  u32x2 b = __builtin_bit_cast(u32x2, __builtin_amdgcn_ds_read_tr16_b64_v4i16((LAS v4i16_t*)(p + step)));
  return u32x4{a.x, a.y, b.x, b.y};
}

DI void dma16(const void* base, u32 voff, u32 lds) {
  u32 keep;
  asm volatile("s_mov_b32 %0, m0\n\ts_mov_b32 m0, %3\n\ts_nop 0\n\tglobal_load_lds_dwordx4 %1, %2\n\ts_mov_b32 m0, %0"
               : "=&s"(keep) : "v"(voff), "s"(base), "s"(lds) : "memory");
}
#define BAR() asm volatile("s_waitcnt lgkmcnt(0)\n\ts_barrier" ::: "memory")
#define DRAIN() asm volatile("s_waitcnt vmcnt(0)" ::: "memory")
DI __amdgpu_buffer_rsrc_t mkrs(const void* p, u32 bytes) { return __builtin_amdgcn_make_buffer_rsrc((void*)p, (short)0, (int)bytes, 0x00020000); }

__global__ void __launch_bounds__(512, 2)
fused_block(const u16* __restrict__ q, const u16* __restrict__ k, const u16* __restrict__ v,
            const u16* __restrict__ shiftk, const u16* __restrict__ ssmk, const u16* __restrict__ ssmD,
            const u16* __restrict__ wr, const u16* __restrict__ wg, const u16* __restrict__ wu,
            const u16* __restrict__ wd, const u16* __restrict__ cvec, const u16* __restrict__ wmod,
            u16* __restrict__ out, char* __restrict__ ws) {
  extern __shared__ __attribute__((aligned(16))) char smem_g[];
  LAS char* smem = (LAS char*)smem_g;
  STAMP(0);
  asm volatile("" ::"s"(q), "s"(k), "s"(v), "s"(shiftk), "s"(ssmk), "s"(ssmD), "s"(wr), "s"(wg), "s"(wu), "s"(wd),
               "s"(cvec), "s"(wmod), "s"(out), "s"(ws));
  const int tid = threadIdx.x, lane = tid & 63;
  const int wave = __builtin_amdgcn_readfirstlane(tid >> 6);
  const int b = blockIdx.x, xcd = b & 7, slot = b >> 3;
  const bool is_conv = slot < 20;
  const int grp = xcd * 20 + slot;
  const int sa_ = slot - 20;
  const int j = is_conv ? grp : 160 + xcd * 12 + sa_;
  const int gu_id = xcd * 7 + sa_, dn_id = xcd * 5 + sa_ - 7, md_id = xcd + 8 * (11 - sa_);
  const bool is_gu = !is_conv && sa_ < 7 && gu_id < 54;
  const bool is_dn = !is_conv && sa_ >= 7;
  const bool is_md = !is_conv && md_id < 60;
  const __amdgpu_buffer_rsrc_t rs = mkrs(ws, OFF_END);
#define LD128(off) __builtin_amdgcn_raw_buffer_load_b128(rs, (off), 0, 16)
#define LD64(off) __builtin_amdgcn_raw_buffer_load_b64(rs, (off), 0, 16)
#define LD32(off) __builtin_amdgcn_raw_buffer_load_b32(rs, (off), 0, 16)
#define ST128(val, off) __builtin_amdgcn_raw_buffer_store_b128((val), rs, (off), 0, 16)
#define ST64(val, off) __builtin_amdgcn_raw_buffer_store_b64((val), rs, (off), 0, 16)
#define ST32(val, off) __builtin_amdgcn_raw_buffer_store_b32((val), rs, (off), 0, 16)
  const u32x4 SENT4 = {SENT, SENT, SENT, SENT};
  const int li = lane & 15, lg = lane >> 4;
  const u32 yin = OFF_YB + (j >> 1) * 20480 + (j & 1) * 64 + (tid >> 2) * 256 + (tid & 3) * 16;

  u32 Dp = 0, wrp = 0, tl, th;
  u32 kvp[2][4];
  u32 qd[8];
  const __amdgpu_buffer_rsrc_t rq = mkrs(q, S_ * D_ * 2);
#define LOADQ() _Pragma("unroll") for (int t = 0; t < 8; ++t) qd[t] = __builtin_amdgcn_raw_buffer_load_b32(rq, tid * 4 + (t % 3) * 1280, j * 10240 + (t / 3) * 3840, 0)
#define MKTAG(e) do { const u64 t_ = (((u64)RFL((e).y) << 32) | RFL((e).x)) + 1; tl = (u32)t_; th = (u32)(t_ >> 32); } while (0)
#define CLEARS() do { ST128(SENT4, yin); ST128(SENT4, yin + 128); ST32(SENT, OFF_CAND + j * 2560 + tid * 4); ST32(SENT, OFF_CAND + j * 2560 + 1280 + tid * 4); } while (0)
#define TAGOK(f) (__all((((f).x ^ tl) | ((f).y ^ th)) == 0))
  if (wave < 5) {
    u32 kk[11], vv[8];
    const __amdgpu_buffer_rsrc_t rk = mkrs(k, S_ * D_ * 2), rv = mkrs(v, S_ * D_ * 2);
    const int r0 = j * 10240, rh = j ? r0 - 3840 : 0;
#pragma unroll
    for (int i = 0; i < 3; ++i) kk[i] = __builtin_amdgcn_raw_buffer_load_b32(rk, tid * 4 + i * 1280, rh, 0);
#pragma unroll
    for (int i = 0; i < 8; ++i) kk[3 + i] = __builtin_amdgcn_raw_buffer_load_b32(rk, tid * 4 + (i % 3) * 1280, r0 + (i / 3) * 3840, 0);
#pragma unroll
    for (int i = 0; i < 8; ++i) vv[i] = __builtin_amdgcn_raw_buffer_load_b32(rv, tid * 4 + (i % 3) * 1280, r0 + (i / 3) * 3840, 0);
    const u32x4 shv = *(const u32x4*)(shiftk + 8 * tid);
    Dp = *(const u32*)(ssmD + 2 * tid);
    wrp = *(const u32*)(wr + 2 * tid);
    const u32x2 e = LD64(OFF_E);
    __builtin_amdgcn_sched_barrier(0);
    if (j == 0) { kk[0] = 0; kk[1] = 0; kk[2] = 0; }
    const f32x2 s0 = {lo16(shv.x), lo16(shv.z)}, s1 = {hi16(shv.x), hi16(shv.z)}, s2 = {lo16(shv.y), lo16(shv.w)}, s3 = {hi16(shv.y), hi16(shv.w)};
    f32x2 kf[11], kv[8];
#pragma unroll
    for (int i = 0; i < 11; ++i) kf[i] = f32x2{lo16(kk[i]), hi16(kk[i])};
#pragma unroll
    for (int t = 0; t < 8; ++t) {
      f32x2 a = kf[t] * s3;
      a = kf[t + 1] * s2 + a;
      a = kf[t + 2] * s1 + a;
      a = kf[t + 3] * s0 + a;
      kv[t] = a * f32x2{lo16(vv[t]), hi16(vv[t])};
    }
#pragma unroll
    for (int d = 0; d < 4; ++d) {
      kvp[0][d] = pkbf(kv[7 - 2 * d].x, kv[6 - 2 * d].x);
      kvp[1][d] = pkbf(kv[7 - 2 * d].y, kv[6 - 2 * d].y);
    }
    const u32 xo = OFF_XT + (tid >> 2) * 32768 + j * 128 + (tid & 3) * 16;
    ST128((u32x4{kvp[0][0], kvp[0][1], kvp[0][2], kvp[0][3]}), xo);
    ST128((u32x4{kvp[1][0], kvp[1][1], kvp[1][2], kvp[1][3]}), xo + 64);
    STAMP(2);
    MKTAG(e);
    DRAIN();
    if (lane == 0) ST64((u32x2{tl, th}), OFF_FLAG + wave * 4096 + j * 16);
    STAMP(3);
  } else if (wave < 7) {
    const u32x2 e = LD64(OFF_E);
    if (is_conv) {
      u32x4 kw[8];
      u32x2 kx[8];
      const __amdgpu_buffer_rsrc_t rK = mkrs(ssmk, S_ * D_ * 2);
      const int e0 = (8 * (grp >> 1) + 4 * (wave - 5) + (grp & 1)) * 4096 + lane * 16;
#pragma unroll
      for (int i = 0; i < 8; ++i) {
        kw[i] = __builtin_amdgcn_raw_buffer_load_b128(rK, e0 + (i >> 2) * 8192 + (i & 3) * 1024, 0, 0);
        kx[i] = __builtin_amdgcn_raw_buffer_load_b64(rK, e0 + (i >> 2) * 8192 + (i & 3) * 1024 + 16, 0, 0);
      }
      __builtin_amdgcn_sched_barrier(0);
#pragma unroll
      for (int i = 0; i < 8; ++i) {
        const int c = (wave - 5) * 2 + (i >> 2), a = (i & 3) * 64 + lane;
        const u32 w0 = kw[i].x, w1 = kw[i].y, w2 = kw[i].z, w3 = kw[i].w, w4 = kx[i].x, w5 = kx[i].y;
        const u32 a0 = __builtin_amdgcn_alignbyte(w1, w0, 2), a1 = __builtin_amdgcn_alignbyte(w2, w1, 2),
                  a2 = __builtin_amdgcn_alignbyte(w3, w2, 2), a3 = __builtin_amdgcn_alignbyte(w4, w3, 2),
                  a4 = __builtin_amdgcn_alignbyte(w5, w4, 2);
        const u32 base = c * 4 * KC_STRIDE + (a + 2) * 16;
        if (i == 0) STAMP(2);
        LDS128(base) = kw[i];
        LDS128(base + KC_STRIDE) = u32x4{a0, a1, a2, a3};
        LDS128(base + 2 * KC_STRIDE) = u32x4{w1, w2, w3, w4};
        LDS128(base + 3 * KC_STRIDE) = u32x4{a1, a2, a3, a4};
        if ((i & 3) == 0 && lane == 0) {
          LDS128(base - 16 + KC_STRIDE) = u32x4{0, 0, 0, w0 << 16};
          LDS128(base - 16 + 2 * KC_STRIDE) = u32x4{0, 0, 0, w0};
          LDS128(base - 16 + 3 * KC_STRIDE) = u32x4{0, 0, w0 << 16, a0};
        }
      }
      STAMP(3);
    }
    MKTAG(e);
  } else {
    const u32x2 e = LD64(OFF_E);
    if (lane == 0) ST64((u32x2{SENT, SENT}), OFF_SCORE + j * 16);
    if (lane < 8) {
      if (is_md) ST128(SENT4, OFF_MOD + md_id * 128 + lane * 16);
      if (is_gu) ST128(SENT4, OFF_A + gu_id * 128 + lane * 16);
    }
    if (is_conv) {
      const u32x4 z = {0, 0, 0, 0};
      if (lane < 60) {
        LDS128(XL + (lane / 30) * XL_STRIDE + (256 + lane % 30) * 16) = z;
        LDS128(XL + (lane / 30 + 2) * XL_STRIDE + (256 + lane % 30) * 16) = z;
      }
      if (lane < 20) LDS128(lane < 16 ? lane * KC_STRIDE : (lane - 16) * 4 * KC_STRIDE + 16) = z;
    }
    MKTAG(e);
    DRAIN();
    if (lane == 0) ST64((u32x2{tl, th}), OFF_F5 + j * 16);
    STAMP(3);
  }
  BAR();
  STAMP(4);

  u32 mp[6] = {SENT, SENT, SENT, SENT, SENT, SENT};
  u32x4 y0 = SENT4, y1 = SENT4;
  if (is_conv) {
    const int c = wave & 3, hf = wave >> 2;
    u32x4 A[8][4];
    const u32 aaddr = (u32)(uintptr_t)smem + (c * 4 + ((li + 1) & 3)) * KC_STRIDE + 16 * lg + 8 * ((li + 1) >> 2) + 256 * hf;
    load_afrags<0>(aaddr, A); load_afrags<2>(aaddr, A); load_afrags<4>(aaddr, A); load_afrags<6>(aaddr, A);
    STAMP(5);
    {
      const u32 fo = (lane < 32 ? OFF_FLAG + (grp >> 5) * 4096 : OFF_F5) + (wave * 32 + (lane & 31)) * 16;
      if (DLY_X) __builtin_amdgcn_s_sleep(DLY_X);
      _Pragma("unroll 1") for (u32 spins = 0; spins < SPIN_CAP; ++spins) {
        CFENCE();
        const u32x2 f = LD64(fo);
        if (TAGOK(f)) break;
        __builtin_amdgcn_s_sleep(2);
      }
      CFENCE();
      STAMP(6);
      const u32 xo = OFF_XT + (grp >> 1) * 32768 + (grp & 1) * 64 + (wave * 32 + (lane >> 2)) * 128 + (lane & 3) * 16;
      const u32x4 x0 = LD128(xo), x1 = LD128(xo + 2048);
      const u32 xa = XL + (lane & 3) * XL_STRIDE + (255 - 32 * wave - (lane >> 2)) * 16;
      LDS128(xa) = x0;
      LDS128(xa - 256) = x1;
    }
    BAR();
    STAMP(7);
    if (wave < 5) { CLEARS(); LOADQ(); }
    const u32 f6o = OFF_F6 + (grp >> 5) * 4096 + ((lane & 16) * 8 + wave * 16 + li) * 16;
    u32x2 f6;
    const u32 bbase = XL + c * XL_STRIDE + (30 + 16 * hf - 2 * li + lg) * 16;
    const u32 pw = hf * 32768 + (lg & 1) * 2048 + (c * 32 + ((2 * li + (lg >> 1) + 4 * c) & 31)) * 16;
    const u32 pr = (tid >> 7) * 4096 + ((tid & 3) * 32 + ((((tid >> 2) & 31) + 4 * (tid & 3)) & 31)) * 16;
    const u32 yo = OFF_YB + (tid >> 3) * 20480 + grp * 128 + (tid & 7) * 16;
    f32x4 acc[8];
#pragma unroll
    for (int n = 0; n < 8; ++n) acc[n] = f32x4{0.f, 0.f, 0.f, 0.f};
    u32x4 B[2][4];
#pragma unroll
    for (int r = 0; r < 4; ++r) B[0][r] = LDS128(bbase + 7 * 512 + 64 * r);
#pragma unroll
    for (int cc = 0; cc < 8; ++cc) {
      if (cc < 7) {
#pragma unroll
        for (int r = 0; r < 4; ++r) B[(cc + 1) & 1][r] = LDS128(bbase + (6 - cc) * 512 + 64 * r);
      }
      __builtin_amdgcn_sched_barrier(0);
#pragma unroll
      for (int r = 0; r < 4; ++r)
#pragma unroll
        for (int n = cc; n < 8; ++n) acc[n] = mfma16(A[n - cc][r], B[cc & 1][r], acc[n]);
      __builtin_amdgcn_sched_barrier(0);
      LDSF4(pw + cc * 4096) = acc[cc];
      if (cc == 0 && wave < 5) {
        asm volatile("s_waitcnt vmcnt(0)" : "+v"(qd[0]), "+v"(qd[1]), "+v"(qd[2]), "+v"(qd[3]), "+v"(qd[4]), "+v"(qd[5]), "+v"(qd[6]), "+v"(qd[7])::"memory");
        if (lane == 0) ST64((u32x2{tl, th}), OFF_F6 + wave * 4096 + j * 16);
      }
      if (cc == 2) f6 = LD64(f6o);
      if (cc == 4) {
        _Pragma("unroll 1") for (u32 spins = 0; spins < SPIN_CAP; ++spins) {
          if (TAGOK(f6)) break;
          __builtin_amdgcn_s_sleep(2);
          CFENCE();
          f6 = LD64(f6o);
        }
      }
      if (cc == 5 && wave < 5) {
#pragma unroll
        for (int i = 0; i < 6; ++i) mp[i] = LD32(OFF_MOD + i * 1280 + tid * 4);
      }
      if (cc == 7 && wave < 5) { y0 = LD128(yin); y1 = LD128(yin + 128); }
      if (cc == 4 || cc == 7) {
        BAR();
        if (cc == 4 && tid == 448) ST64((u32x2{tl, th}), OFF_P + grp * 128);
        const u32 p0 = pr + (cc == 7 ? 16384 : 0);
        const f32x4 lo = LDSF4(p0) + LDSF4(p0 + 32768), hi = LDSF4(p0 + 2048) + LDSF4(p0 + 32768 + 2048);
        ST128((u32x4{san(pkbf(lo.x, lo.y)), san(pkbf(lo.z, lo.w)), san(pkbf(hi.x, hi.y)), san(pkbf(hi.z, hi.w))}),
              yo + (cc == 7 ? 64 * 20480 : 0));
      }
    }
    STAMP(8);
  } else {
    if (wave < 5) {
      __builtin_amdgcn_s_sleep(DLY_C);
      CLEARS();
      LOADQ();
      const u32 fo = OFF_F5 + (tid & 255) * 16;
      _Pragma("unroll 1") for (u32 spins = 0; spins < SPIN_CAP; ++spins) {
        CFENCE();
        const u32x2 f = LD64(fo);
        if (TAGOK(f)) break;
        __builtin_amdgcn_s_sleep(4);
      }
      DRAIN();
      if (lane == 0) ST64((u32x2{tl, th}), OFF_F6 + wave * 4096 + j * 16);
      STAMP(6);
    } else if (is_md) {
      __builtin_amdgcn_s_sleep(DLY_W);
      const int wv = wave - 5;
      const int G = wv * 8 + (lane >> 3);
      const __amdgpu_buffer_rsrc_t rm = mkrs(wmod, 640 * 3840 * 2), rc = mkrs(cvec, 1280);
      u32 cv[4];
#pragma unroll
      for (int u = 0; u < 4; ++u) cv[u] = __builtin_amdgcn_raw_buffer_load_b16(rc, (G * 27 + (lane & 7) + 8 * u) * 2, 0, 0);
      u32x4 wm[27];
      const u32 mo = G * 27 * 7680 + md_id * 128 + (lane & 7) * 16;
#pragma unroll
      for (int i = 0; i < 27; ++i) wm[i] = __builtin_amdgcn_raw_buffer_load_b128(rm, (i >= 19 && G == 23) ? 0x7000000u : mo, i * 7680, 0);
#pragma unroll
      for (int u = 0; u < 4; ++u) {
        const float x = lo16(cv[u]);
        if (u < 3 || (lane & 7) < 4) *(LAS float*)(smem + SIL + (G * 28 + (lane & 7) + 8 * u) * 4) = rbf(x * sigm(x));
      }
      f32x2 a[4];
#pragma unroll
      for (int e = 0; e < 4; ++e) a[e] = f32x2{0.f, 0.f};
#pragma unroll
      for (int i4 = 0; i4 < 7; ++i4) {
        const f32x4 s4 = LDSF4(SIL + (G * 28 + 4 * i4) * 4);
#pragma unroll
        for (int u = 0; u < 4; ++u)
          if (4 * i4 + u < 27) {
#pragma unroll
            for (int e = 0; e < 4; ++e) a[e] += f32x2{lo16(wm[4 * i4 + u][e]), hi16(wm[4 * i4 + u][e])} * s4[u];
          }
      }
      f32x2 y2;
      y2.x = swap16_add(swap32_add(a[0].x, a[2].x), swap32_add(a[1].x, a[3].x));
      y2.y = swap16_add(swap32_add(a[0].y, a[2].y), swap32_add(a[1].y, a[3].y));
      y2.x += dppf<0x128>(y2.x); y2.y += dppf<0x128>(y2.y);
      if ((lane & 8) == 0) *(LAS f32x2*)(smem + RR + wv * 256 + (lane & 7) * 32 + lg * 8) = y2;
      STAMP(6);
    }
    STAMP(7);
  }

  f32x2 qf[8], cb1, sb, gb;
  if (wave < 5) {
    float V[8][2];
#pragma unroll
    for (int t = 0; t < 8; ++t) {
      qf[t] = f32x2{lo16(qd[t]), hi16(qd[t])};
      V[t][0] = qf[t].x + qf[t].y;
      V[t][1] = qf[t].x * qf[t].x + qf[t].y * qf[t].y;
    }
    reduce_tok<2>(V, 0.f, smem, RED1 + wave * 128, lane);
  }
#pragma unroll 1
  for (int pass = 0; pass < 2; ++pass) {
    if (pass == 1) {
      BAR();
      if (is_md && wave == 5) {
        const f32x2 s = *(LAS f32x2*)(smem + RR + (lane & 31) * 8) + *(LAS f32x2*)(smem + RR + 256 + (lane & 31) * 8) +
                        *(LAS f32x2*)(smem + RR + 512 + (lane & 31) * 8);
        if (lane < 32) ST32(san(pkbf(s.x, s.y)), OFF_MOD + md_id * 128 + lane * 4);
      }
    }
    if (wave >= 5 && (pass == 1) == is_md) {
      const u32 lb = (u32)(uintptr_t)smem;
      const int wv = wave - 5;
      if (is_gu) {
        if (pass == 0) __builtin_amdgcn_s_sleep(DLY_W);
        u32 vo = ((lane >> 2) + 16 * wv) * 3456 + gu_id * 64 + (lane & 3) * 16;
        for (int i = wv; i < 40; i += 3, vo += 48 * 3456) { dma16(wg, vo, lb + i * 1024); dma16(wu, vo, lb + UPB + i * 1024); }
      } else if (is_dn) {
        u32 vo = ((lane >> 1) + 32 * wv) * 1280 + dn_id * 32 + (lane & 1) * 16;
        for (int i = wv; i < 54; i += 3, vo += 96 * 1280) dma16(wd, vo, lb + i * 1024);
      }
    }
  }
  STAMP(9);
  f32x2 x1[8];
  if (wave < 5) {
    f32x2 s = *(LAS f32x2*)(smem + RED1 + (lane & 7) * 16);
#pragma unroll
    for (int w = 1; w < 5; ++w) s += *(LAS f32x2*)(smem + RED1 + w * 128 + (lane & 7) * 16);
    const float m = s.x * (1.f / D_);
    const float rstd = __builtin_amdgcn_rsqf(fmaxf(s.y * (1.f / D_) - m * m, 0.f) + 1e-5f);
    _Pragma("unroll 1") for (u32 spins = 0; spins < SPIN_CAP; ++spins) {
      if (__all(umax2(umax2(umax2(mp[0], mp[1]), umax2(mp[2], mp[3])), umax2(mp[4], mp[5])) != SENT)) break;
      if (spins) __builtin_amdgcn_s_sleep(8);
      CFENCE();
#pragma unroll
      for (int i = 0; i < 6; ++i) mp[i] = LD32(OFF_MOD + i * 1280 + tid * 4);
    }
    CFENCE();
    const f32x2 sa = {lo16(mp[0]), hi16(mp[0])}, ga = {lo16(mp[2]), hi16(mp[2])};
    f32x2 ca1;
    { const u32 t = pkbf(1.f + lo16(mp[1]), 1.f + hi16(mp[1])); ca1 = f32x2{lo16(t), hi16(t)}; }
    sb = f32x2{lo16(mp[3]), hi16(mp[3])};
    { const u32 t = pkbf(1.f + lo16(mp[4]), 1.f + hi16(mp[4])); cb1 = f32x2{lo16(t), hi16(t)}; }
    gb = f32x2{lo16(mp[5]), hi16(mp[5])};
    const f32x2 wrf = {lo16(wrp), hi16(wrp)}, gw = cb1 * wrf, Df = {lo16(Dp), hi16(Dp)};
    f32x2 hg[8], kd[8];
#pragma unroll
    for (int t = 0; t < 8; ++t) {
      const float mt = rdl(m, t), rt = rdl(rstd, t);
      hg[t] = ((qf[t] - mt) * rt * ca1 + sa) * ga;
      const u32 u0 = kvp[0][(7 - t) >> 1], u1 = kvp[1][(7 - t) >> 1];
      kd[t] = ((t & 1) ? f32x2{lo16(u0), lo16(u1)} : f32x2{hi16(u0), hi16(u1)}) * Df * hg[t] + qf[t];
    }
    float ex = swap32_add(gw.x + gw.y, sb.x * wrf.x + sb.y * wrf.y);
    ex = swap16_add(ex, ex);
    asm volatile("" : "+v"(ex));
    STAMP(10);
    if (!is_conv) {
      const u32 po = OFF_P + ((j - 160) * 5 / 3) * 128;
      _Pragma("unroll 1") for (u32 spins = 0; spins < SPIN_CAP; ++spins) {
        CFENCE();
        const u32x2 f = LD64(po);
        if (TAGOK(f)) break;
        __builtin_amdgcn_s_sleep(4);
      }
      __builtin_amdgcn_s_sleep(DLY_Y);
    }
    _Pragma("unroll 1") for (u32 spins = 0; spins < SPIN_CAP; ++spins) {
      if (__all(umax2(umax4(y0), umax4(y1)) != SENT)) break;
      if (spins) __builtin_amdgcn_s_sleep(2);
      CFENCE();
      y0 = LD128(yin); y1 = LD128(yin + 128);
    }
    DRAIN();
    STAMP(11);
    float V[8][3];
#pragma unroll
    for (int t = 0; t < 8; ++t) {
      const u32 u0 = y0[t >> 1], u1 = y1[t >> 1];
      x1[t] = hg[t] * ((t & 1) ? f32x2{hi16(u0), hi16(u1)} : f32x2{lo16(u0), lo16(u1)}) + kd[t];
      V[t][0] = x1[t].x + x1[t].y;
      V[t][1] = x1[t].x * x1[t].x + x1[t].y * x1[t].y;
      V[t][2] = x1[t].x * gw.x + x1[t].y * gw.y;
    }
    reduce_tok<3>(V, ex, smem, RED2 + wave * 128, lane);
  } else {
    DRAIN();
  }
  BAR();
  STAMP(12);
  const __amdgpu_buffer_rsrc_t ro = mkrs(out, S_ * D_ * 2);
  u32 w0 = 0, w1 = 0;
  u32 ov = 0, gv = 0;
  if (wave < 6) {
    const f32x4 s = gather5(smem, RED2, lane);
    const float Gs = rdl(s.w, 0), c0s = rdl(s.w, 4);
    const float m = s.x * (1.f / D_);
    const float rstd = __builtin_amdgcn_rsqf(fmaxf(s.y * (1.f / D_) - m * m, 0.f) + 1e-5f);
    const float r = rstd * (s.z - m * Gs) + c0s;
    u32 ob = __float_as_uint(r);
    ob ^= (u32)((int)ob >> 31) | 0x80000000u;
    ob = ob < 0xFFFFE000u ? ob : 0xFFFFE000u;
    const u32 key = (ob & 0xFFFFF000u) | ((2047u - (8 * j + (lane & 7))) << 1);
    u32 mx = umax2(key, dppu<0x124>(key)); mx = umax2(mx, dppu<0x122>(mx)); mx = umax2(mx, dppu<0x121>(mx));
    u32 k2 = key == mx ? 0u : key;
    k2 = umax2(k2, dppu<0x124>(k2)); k2 = umax2(k2, dppu<0x122>(k2)); k2 = umax2(k2, dppu<0x121>(k2));
    const u32 kA = RFL(mx), kB = RFL(k2) | 1u;
    if (wave == 5) {
      if (lane == 0) ST64((u32x2{kA, kB}), OFF_SCORE + j * 16);
      if (is_gu || is_dn) {
        __builtin_amdgcn_s_sleep(DLY_SC);
        u32x2 g[4];
        _Pragma("unroll 1") for (u32 spins = 0; spins < SPIN_CAP; ++spins) {
          CFENCE();
#pragma unroll
          for (int i = 0; i < 4; ++i) g[i] = LD64(OFF_SCORE + lane * 16 + i * 1024);
          if (__all(umax2(umax2(umax2(g[0].x, g[0].y), umax2(g[1].x, g[1].y)), umax2(umax2(g[2].x, g[2].y), umax2(g[3].x, g[3].y))) != SENT)) break;
          __builtin_amdgcn_s_sleep(2);
        }
        CFENCE();
        STAMP(13);
        w0 = RFL(wave_umax(umax2(umax2(g[0].x, g[1].x), umax2(g[2].x, g[3].x))));
        u32 c2 = 0;
#pragma unroll
        for (int i = 0; i < 4; ++i) c2 = umax2(c2, g[i].x == w0 ? g[i].y : g[i].x);
        w1 = RFL(wave_umax(c2));
        const u32 wk = lane < 32 ? w0 : w1;
        const u32 tok = 2047 - ((wk >> 1) & 2047);
        if (is_gu) {
          const u32 l5 = lane & 31, o2 = 1024 + (l5 & 15) * 16;
          const u32 row = OFF_CAND + (tok >> 3) * 2560 + (wk & 1) * 1280;
          u32x4 h0, h1, h2;
          _Pragma("unroll 1") for (u32 spins = 0; spins < SPIN_CAP; ++spins) {
            CFENCE();
            h0 = LD128(row + l5 * 16); h1 = LD128(row + 512 + l5 * 16); h2 = LD128(row + o2);
            if (__all(umax2(umax2(umax4(h0), umax4(h1)), umax4(h2)) != SENT)) break;
            __builtin_amdgcn_s_sleep(1);
          }
          CFENCE();
          const u32 hb = HR + (lane >> 5) * HR_STRIDE;
          LDS128(hb + l5 * 16) = h0; LDS128(hb + 512 + l5 * 16) = h1; LDS128(hb + o2) = h2;
        } else {
          if (dn_id == 0 && lane == 0) ST64((u32x2{tl, th}), OFF_E);
          _Pragma("unroll 1") for (u32 spins = 0; spins < SPIN_CAP; ++spins) {
            CFENCE();
            const u32x2 f = LD64(OFF_RD + (tok >> 3) * 64 + (dn_id >> 3) * 8);
            if (TAGOK(f)) break;
            __builtin_amdgcn_s_sleep(1);
          }
          CFENCE();
          ov = __builtin_amdgcn_raw_buffer_load_b16(ro, tok * 1280 + dn_id * 32 + li * 2, 0, 16);
          gv = __builtin_amdgcn_raw_buffer_load_b16(rs, OFF_MOD + 5 * 1280 + dn_id * 32 + li * 2, 0, 16);
        }
      }
    } else {
      const int tA = (2047 - ((kA >> 1) & 2047)) & 7, tB = (2047 - ((kB >> 1) & 2047)) & 7;
      u32 d0 = 0, d1 = 0, op[8];
#pragma unroll
      for (int t = 0; t < 8; ++t) {
        const float mt = rdl(m, t), rt = rdl(rstd, t);
        const f32x2 h2 = (x1[t] - mt) * rt * cb1 + sb;
        const f32x2 o = gb * h2 + x1[t];
        op[t] = pkbf(o.x, o.y);
        const u32 hp = pkbf(h2.x, h2.y);
        d0 = t == tA ? hp : d0; d1 = t == tB ? hp : d1;
      }
      ST32(san(d0), OFF_CAND + j * 2560 + tid * 4);
      ST32(san(d1), OFF_CAND + j * 2560 + 1280 + tid * 4);
#pragma unroll
      for (int t = 0; t < 8; ++t) {
        if (OUT_AUX == 16 || t == tA || t == tB) __builtin_amdgcn_raw_buffer_store_b32(op[t], ro, tid * 4 + (t % 3) * 1280, j * 10240 + (t / 3) * 3840, 16);
        else __builtin_amdgcn_raw_buffer_store_b32(op[t], ro, tid * 4 + (t % 3) * 1280, j * 10240 + (t / 3) * 3840, OUT_AUX);
      }
      if (!is_gu && !is_dn) {
        DRAIN();
        if (lane == 0) ST64((u32x2{tl, th}), OFF_RD + j * 64 + wave * 8);
      }
    }
  }
  if (is_gu) {
    u32x4 Wf[20];
    if (wave < 4) {
      LAS char* p = smem + ((li >> 1) & 1) * UPB + (8 * lg + (li >> 2)) * 64 + wave * 16 + (li & 1) * 8;
#pragma unroll
      for (int s = 0; s < 20; ++s) Wf[s] = tr_frag(p + s * 2048, 256);
    }
    if (wave < 5) {
      DRAIN();
      if (lane == 0) ST64((u32x2{tl, th}), OFF_RD + j * 64 + wave * 8);
    }
    BAR();
    STAMP(14);
    if (wave < 4) {
      f32x4 a0 = {0.f, 0.f, 0.f, 0.f}, a1 = a0;
      const u32 hb = HR + (li & 1) * HR_STRIDE + lg * 16;
#pragma unroll
      for (int s = 0; s < 20; s += 2) {
        a0 = mfma16(Wf[s], LDS128(hb + s * 64), a0);
        a1 = mfma16(Wf[s + 1], LDS128(hb + s * 64 + 64), a1);
      }
      a0 += a1;
      float av[4];
#pragma unroll
      for (int e = 0; e < 4; ++e) {
        auto r = __builtin_amdgcn_permlane32_swap(__float_as_uint(a0[e]), __float_as_uint(a0[e]), false, false);
        const float g = rbf(__uint_as_float(r[0])), u = rbf(__uint_as_float(r[1]));
        av[e] = rbf(g * sigm(g)) * u;
      }
      if (li < 2 && lg < 2) ST64((u32x2{san(pkbf(av[0], av[1])), san(pkbf(av[2], av[3]))}), OFF_A + gu_id * 128 + li * 64 + wave * 16 + lg * 8);
    }
    STAMP(15);
  } else if (is_dn) {
    u32x4 Wf[7];
    LAS char* p = smem + (32 * wave + 8 * lg + (li >> 2)) * 32 + (li & 3) * 8;
#pragma unroll
    for (int s = 0; s < 6; ++s) Wf[s] = tr_frag(p + s * 8192, 128);
    Wf[6] = tr_frag(p + (wave < 6 ? 6 * 8192 : 0), 128);
    if (wave < 5) {
      DRAIN();
      if (lane == 0) ST64((u32x2{tl, th}), OFF_RD + j * 64 + wave * 8);
    }
    BAR();
    __builtin_amdgcn_s_sleep(DLY_A);
    u32x4 Bf[7];
    const u32 ao = OFF_A + wave * 128 + (li & 1) * 64 + lg * 16;
    _Pragma("unroll 1") for (u32 spins = 0; spins < SPIN_CAP; ++spins) {
      CFENCE();
      u32 mx = 0;
#pragma unroll
      for (int s = 0; s < 6; ++s) Bf[s] = LD128(ao + s * 1024);
      Bf[6] = LD128(ao + (wave < 6 ? 6 * 1024 : 0));
#pragma unroll
      for (int s = 0; s < 7; ++s) mx = umax2(mx, umax4(Bf[s]));
      if (__all(mx != SENT)) break;
      __builtin_amdgcn_s_sleep(1);
    }
    CFENCE();
    STAMP(14);
    f32x4 a0 = {0.f, 0.f, 0.f, 0.f}, a1 = a0;
#pragma unroll
    for (int s = 0; s < 6; s += 2) { a0 = mfma16(Wf[s], Bf[s], a0); a1 = mfma16(Wf[s + 1], Bf[s + 1], a1); }
    if (wave < 6) a0 = mfma16(Wf[6], Bf[6], a0);
    if (li < 2) LDSF4(RR + ((wave * 2 + li) * 16 + 4 * lg) * 4) = a0 + a1;
    BAR();
    if (wave == 5) {
      float s = 0.f;
#pragma unroll
      for (int w = 0; w < 8; ++w) s += *(LAS float*)(smem + RR + w * 128 + (lane >> 5) * 64 + li * 4);
      const u32 wk = lane < 32 ? w0 : w1, kb = wk & 0xFFFFF000u;
      const float rv = rbf(__uint_as_float((kb & 0x80000000u) ? (kb ^ 0x80000000u) : ~kb));
      const float y = rbf(rbf(s) * rbf(sigm(rv)));
      const float o = lo16(ov) + lo16(gv) * y;
      if ((lane & 16) == 0) __builtin_amdgcn_raw_buffer_store_b16((u16)pkbf(o, o), ro, (2047 - ((wk >> 1) & 2047)) * 1280 + dn_id * 32 + li * 2, 0, 16);
    }
    STAMP(15);
  }
}

extern "C" void kernel_launch(void* const* d_in, const int* in_sizes, int n_in, void* d_out, int out_size,
                              void* d_ws, size_t ws_size, hipStream_t stream) {
  (void)in_sizes; (void)n_in; (void)out_size; (void)ws_size;
  (void)hipFuncSetAttribute((const void*)fused_block, hipFuncAttributeMaxDynamicSharedMemorySize, (int)LDS_BYTES);
  fused_block<<<256, 512, LDS_BYTES, stream>>>(
      (const u16*)d_in[0], (const u16*)d_in[1], (const u16*)d_in[2], (const u16*)d_in[3], (const u16*)d_in[4],
      (const u16*)d_in[5], (const u16*)d_in[7], (const u16*)d_in[8], (const u16*)d_in[9], (const u16*)d_in[10],
      (const u16*)d_in[11], (const u16*)d_in[12], (u16*)d_out, (char*)d_ws);
}
```

```cpp
#include <hip/hip_runtime.h>
#include <hip/hip_bf16.h>
#include <stdint.h>

typedef unsigned short u16;
typedef unsigned int u32;
typedef unsigned long long u64;
typedef u32 u32x2 __attribute__((ext_vector_type(2)));
typedef u32 u32x4 __attribute__((ext_vector_type(4)));
typedef float f32x2 __attribute__((ext_vector_type(2)));
typedef float f32x4 __attribute__((ext_vector_type(4)));
typedef short s16x4 __attribute__((ext_vector_type(4)));
typedef __bf16 bf16x2 __attribute__((ext_vector_type(2)));
typedef __bf16 bf16x8 __attribute__((ext_vector_type(8)));
#define LAS __attribute__((address_space(3)))
#define GAS __attribute__((address_space(1)))
#define RLX_AGENT __ATOMIC_RELAXED, __HIP_MEMORY_SCOPE_AGENT
#define DI __device__ __forceinline__

constexpr int S_ = 2048, D_ = 640, F_ = 1728;
constexpr u32 SENT = 0xFFFFFFFFu;
constexpr u32 SPIN_CAP = 1u << 16;
constexpr u32 OFF_E = 0, OFF_FLAG = 4096, OFF_F5 = OFF_FLAG + 5 * 4096, OFF_F6 = OFF_F5 + 4096, OFF_SCORE = OFF_F6 + 5 * 4096,
              OFF_MOD = OFF_SCORE + 4096, OFF_A = OFF_MOD + 8192, OFF_RD = OFF_A + 8192, OFF_P = OFF_RD + 16384;
constexpr u32 OFF_XT = 131072, OFF_YB = OFF_XT + 2621440, OFF_CAND = OFF_YB + 2621440, OFF_END = OFF_CAND + 655360;
constexpr u32 OFF_DBG = 8u << 20;
constexpr u32 KC_STRIDE = 4160, XL = 66560, XL_STRIDE = 4640, UPB = 40960 + 32, RED1 = 86016, RED2 = RED1 + 640, RR = RED2 + 640,
              TAGW = RR + 1024, WINW = TAGW + 16, SIL = WINW + 16, HR = SIL + 2688, HR_STRIDE = 1344, LDS_BYTES = HR + 2 * HR_STRIDE;
#define DLY_Y 10
#define DLY_SC 16
#define DLY_A 16
#define DLY_W 16
#define DLY_C 24
#define DLY_X 0
#define OUT_AUX 16

#define STAMP(i) do {} while (0)

DI float lo16(u32 x) { return __uint_as_float(x << 16); }
DI float hi16(u32 x) { return __uint_as_float(x & 0xffff0000u); }
DI u32 pkbf(float lo, float hi) { f32x2 f = {lo, hi}; return __builtin_bit_cast(u32, __builtin_convertvector(f, bf16x2)); }
DI float rbf(float x) { return lo16(pkbf(x, x)); }
DI u32 san(u32 x) { return x < 0xFFFFFFFEu ? x : 0xFFFFFFFEu; }
DI u32 umax2(u32 a, u32 b) { return a > b ? a : b; }
DI u32 umax4(u32x4 v) { return umax2(umax2(v.x, v.y), umax2(v.z, v.w)); }
DI float sigm(float x) { return __builtin_amdgcn_rcpf(1.f + __expf(-x)); }
template <int CTRL> DI u32 dppu(u32 x) { return (u32)__builtin_amdgcn_update_dpp(0, (int)x, CTRL, 0xf, 0xf, false); }
template <int CTRL> DI float dppf(float x) { return __uint_as_float(dppu<CTRL>(__float_as_uint(x))); }
DI float swap32_add(float a, float b) { auto r = __builtin_amdgcn_permlane32_swap(__float_as_uint(a), __float_as_uint(b), false, false); return __uint_as_float(r[0]) + __uint_as_float(r[1]); }
DI float swap16_add(float a, float b) { auto r = __builtin_amdgcn_permlane16_swap(__float_as_uint(a), __float_as_uint(b), false, false); return __uint_as_float(r[0]) + __uint_as_float(r[1]); }
DI float row_sum(float x) { x += dppf<0x128>(x); x += dppf<0x124>(x); x += dppf<0x122>(x); x += dppf<0x121>(x); return x; }
DI u32 wave_umax(u32 x) {
  x = umax2(x, dppu<0x128>(x)); x = umax2(x, dppu<0x124>(x)); x = umax2(x, dppu<0x122>(x)); x = umax2(x, dppu<0x121>(x));
  auto r = __builtin_amdgcn_permlane16_swap(x, x, false, false); x = umax2(r[0], r[1]);
  auto s = __builtin_amdgcn_permlane32_swap(x, x, false, false); return umax2(s[0], s[1]);
}
DI f32x4 mfma16(u32x4 a, u32x4 b, f32x4 c) { return __builtin_amdgcn_mfma_f32_16x16x32_bf16(__builtin_bit_cast(bf16x8, a), __builtin_bit_cast(bf16x8, b), c, 0, 0, 0); }
#define LDS128(off) (*(LAS u32x4*)(smem + (off)))
#define LDSF4(off) (*(LAS f32x4*)(smem + (off)))
#define LDS32(off) (*(LAS u32*)(smem + (off)))
#define RFL(x) ((u32)__builtin_amdgcn_readfirstlane((int)(x)))
#define CFENCE() asm volatile("" ::: "memory")

#define RS_STEP(n) "v_add_f32_dpp %0, %0, %0 row_ror:" #n " row_mask:0xf bank_mask:0xf\n\tv_add_f32_dpp %1, %1, %1 row_ror:" #n " row_mask:0xf bank_mask:0xf\n\t" \
                   "v_add_f32_dpp %2, %2, %2 row_ror:" #n " row_mask:0xf bank_mask:0xf\n\tv_add_f32_dpp %3, %3, %3 row_ror:" #n " row_mask:0xf bank_mask:0xf\n\t"
#define RS_STEP3(n) "v_add_f32_dpp %4, %4, %4 row_ror:" #n " row_mask:0xf bank_mask:0xf\n\tv_add_f32_dpp %5, %5, %5 row_ror:" #n " row_mask:0xf bank_mask:0xf\n\t" \
                    "v_add_f32_dpp %6, %6, %6 row_ror:" #n " row_mask:0xf bank_mask:0xf\n\t"
template <int NQ>
DI void reduce_tok(const float (&V)[8][NQ], float ex, LAS char* smem, u32 dst, int lane) {
  float W[4][NQ], X[2][NQ];
#pragma unroll
  for (int t = 0; t < 4; ++t)
#pragma unroll
    for (int q = 0; q < NQ; ++q) W[t][q] = swap32_add(V[t][q], V[t + 4][q]);
#pragma unroll
  for (int t = 0; t < 2; ++t)
#pragma unroll
    for (int q = 0; q < NQ; ++q) X[t][q] = swap16_add(W[t][q], W[t + 2][q]);
  const int sel = lane & 15;
  const u32 ad = dst + (sel + 2 * (lane >> 4)) * 16;
  if constexpr (NQ == 2) {
    asm volatile("s_nop 1\n\t" RS_STEP(8) RS_STEP(4) RS_STEP(2) RS_STEP(1) "s_nop 0"
                 : "+v"(X[0][0]), "+v"(X[0][1]), "+v"(X[1][0]), "+v"(X[1][1]));
    if (sel < 2) *(LAS f32x2*)(smem + ad) = f32x2{sel ? X[1][0] : X[0][0], sel ? X[1][1] : X[0][1]};
  } else {
    asm volatile("s_nop 1\n\t" RS_STEP(8) RS_STEP3(8) RS_STEP(4) RS_STEP3(4) RS_STEP(2) RS_STEP3(2) RS_STEP(1) RS_STEP3(1) "s_nop 0"
                 : "+v"(X[0][0]), "+v"(X[0][1]), "+v"(X[1][0]), "+v"(X[1][1]), "+v"(X[0][2]), "+v"(X[1][2]), "+v"(ex));
    if (sel < 2) LDSF4(ad) = f32x4{sel ? X[1][0] : X[0][0], sel ? X[1][1] : X[0][1], sel ? X[1][2] : X[0][2], ex};
  }
}
DI f32x4 gather5(LAS char* smem, u32 base, int lane) {
  f32x4 s = LDSF4(base + (lane & 7) * 16);
#pragma unroll
  for (int w = 1; w < 5; ++w) s += LDSF4(base + w * 128 + (lane & 7) * 16);
  return s;
}
DI float rdl(float x, int l) { return __uint_as_float((u32)__builtin_amdgcn_readlane((int)__float_as_uint(x), l)); }

template <int M>
DI void load_afrags(u32 addr, u32x4 (&A)[8][4]) {
  u32x2 o[16];
  asm volatile(
      "ds_read_b64 %0, %16 offset:%17\n\tds_read_b64 %1, %16 offset:%17+8\n\t"
      "ds_read_b64 %2, %16 offset:%17+64\n\tds_read_b64 %3, %16 offset:%17+72\n\t"
      "ds_read_b64 %4, %16 offset:%17+128\n\tds_read_b64 %5, %16 offset:%17+136\n\t"
      "ds_read_b64 %6, %16 offset:%17+192\n\tds_read_b64 %7, %16 offset:%17+200\n\t"
      "ds_read_b64 %8, %16 offset:%17+512\n\tds_read_b64 %9, %16 offset:%17+520\n\t"
      "ds_read_b64 %10, %16 offset:%17+576\n\tds_read_b64 %11, %16 offset:%17+584\n\t"
      "ds_read_b64 %12, %16 offset:%17+640\n\tds_read_b64 %13, %16 offset:%17+648\n\t"
      "ds_read_b64 %14, %16 offset:%17+704\n\tds_read_b64 %15, %16 offset:%17+712\n\t"
      "s_waitcnt lgkmcnt(0)"
      : "=&v"(o[0]), "=&v"(o[1]), "=&v"(o[2]), "=&v"(o[3]), "=&v"(o[4]), "=&v"(o[5]), "=&v"(o[6]), "=&v"(o[7]),
        "=&v"(o[8]), "=&v"(o[9]), "=&v"(o[10]), "=&v"(o[11]), "=&v"(o[12]), "=&v"(o[13]), "=&v"(o[14]), "=&v"(o[15])
      : "v"(addr), "i"(M * 512)
      : "memory");
#pragma unroll
  for (int r = 0; r < 4; ++r) {
    A[M][r] = u32x4{o[2 * r].x, o[2 * r].y, o[2 * r + 1].x, o[2 * r + 1].y};
    A[M + 1][r] = u32x4{o[8 + 2 * r].x, o[8 + 2 * r].y, o[8 + 2 * r + 1].x, o[8 + 2 * r + 1].y};
  }
}
DI u32x4 tr_frag(LAS char* p, u32 step) {
  typedef short v4i16_t __attribute__((ext_vector_type(4)));
  u32x2 a = __builtin_bit_cast(u32x2, __builtin_amdgcn_ds_read_tr16_b64_v4i16((LAS v4i16_t*)p));
  u32x2 b = __builtin_bit_cast(u32x2, __builtin_amdgcn_ds_read_tr16_b64_v4i16((LAS v4i16_t*)(p + step)));
  return u32x4{a.x, a.y, b.x, b.y};
}

DI void dma16(const void* base, u32 voff, u32 lds) {
  u32 keep;
  asm volatile("s_mov_b32 %0, m0\n\ts_mov_b32 m0, %3\n\ts_nop 0\n\tglobal_load_lds_dwordx4 %1, %2\n\ts_mov_b32 m0, %0"
               : "=&s"(keep) : "v"(voff), "s"(base), "s"(lds) : "memory");
}
#define BAR() asm volatile("s_waitcnt lgkmcnt(0)\n\ts_barrier" ::: "memory")
#define DRAIN() asm volatile("s_waitcnt vmcnt(0)" ::: "memory")
DI __amdgpu_buffer_rsrc_t mkrs(const void* p, u32 bytes) { return __builtin_amdgcn_make_buffer_rsrc((void*)p, (short)0, (int)bytes, 0x00020000); }

__global__ void __launch_bounds__(512, 2)
fused_block(const u16* __restrict__ q, const u16* __restrict__ k, const u16* __restrict__ v,
            const u16* __restrict__ shiftk, const u16* __restrict__ ssmk, const u16* __restrict__ ssmD,
            const u16* __restrict__ wr, const u16* __restrict__ wg, const u16* __restrict__ wu,
            const u16* __restrict__ wd, const u16* __restrict__ cvec, const u16* __restrict__ wmod,
            u16* __restrict__ out, char* __restrict__ ws) {
  extern __shared__ __attribute__((aligned(16))) char smem_g[];
  LAS char* smem = (LAS char*)smem_g;
  const u64 tstart = __builtin_amdgcn_s_memrealtime();
  asm volatile("" ::"s"(q), "s"(k), "s"(v), "s"(shiftk), "s"(ssmk), "s"(ssmD), "s"(wr), "s"(wg), "s"(wu), "s"(wd),
               "s"(cvec), "s"(wmod), "s"(out), "s"(ws));
  const int tid = threadIdx.x, lane = tid & 63;
  const int wave = __builtin_amdgcn_readfirstlane(tid >> 6);
  const int b = blockIdx.x, xcd = b & 7, slot = b >> 3;
  const bool is_conv = slot < 20;
  const int grp = xcd * 20 + slot;
  const int sa_ = slot - 20;
  const int j = is_conv ? grp : 160 + xcd * 12 + sa_;
  const int gu_id = xcd * 7 + sa_, dn_id = xcd * 5 + sa_ - 7, md_id = xcd + 8 * (11 - sa_);
  const bool is_gu = !is_conv && sa_ < 7 && gu_id < 54;
  const bool is_dn = !is_conv && sa_ >= 7;
  const bool is_md = !is_conv && md_id < 60;
  const __amdgpu_buffer_rsrc_t rs = mkrs(ws, OFF_END);
#define LD128(off) __builtin_amdgcn_raw_buffer_load_b128(rs, (off), 0, 16)
#define LD64(off) __builtin_amdgcn_raw_buffer_load_b64(rs, (off), 0, 16)
#define LD32(off) __builtin_amdgcn_raw_buffer_load_b32(rs, (off), 0, 16)
#define ST128(val, off) __builtin_amdgcn_raw_buffer_store_b128((val), rs, (off), 0, 16)
#define ST64(val, off) __builtin_amdgcn_raw_buffer_store_b64((val), rs, (off), 0, 16)
#define ST32(val, off) __builtin_amdgcn_raw_buffer_store_b32((val), rs, (off), 0, 16)
  const u32x4 SENT4 = {SENT, SENT, SENT, SENT};
  const int li = lane & 15, lg = lane >> 4;
  const u32 yin = OFF_YB + (j >> 1) * 20480 + (j & 1) * 64 + (tid >> 2) * 256 + (tid & 3) * 16;

  u32 Dp = 0, wrp = 0, tl, th;
  u32 kvp[2][4];
  u32 qd[8];
  const __amdgpu_buffer_rsrc_t rq = mkrs(q, S_ * D_ * 2);
#define LOADQ() _Pragma("unroll") for (int t = 0; t < 8; ++t) qd[t] = __builtin_amdgcn_raw_buffer_load_b32(rq, tid * 4 + (t % 3) * 1280, j * 10240 + (t / 3) * 3840, 0)
#define MKTAG(e) do { const u64 t_ = (((u64)RFL((e).y) << 32) | RFL((e).x)) + 1; tl = (u32)t_; th = (u32)(t_ >> 32); } while (0)
#define CLEARS() do { ST128(SENT4, yin); ST128(SENT4, yin + 128); ST32(SENT, OFF_CAND + j * 2560 + tid * 4); ST32(SENT, OFF_CAND + j * 2560 + 1280 + tid * 4); } while (0)
#define TAGOK(f) (__all((((f).x ^ tl) | ((f).y ^ th)) == 0))
  if (wave < 5) {
    u32 kk[11], vv[8];
    const __amdgpu_buffer_rsrc_t rk = mkrs(k, S_ * D_ * 2), rv = mkrs(v, S_ * D_ * 2);
    const int r0 = j * 10240, rh = j ? r0 - 3840 : 0;
#pragma unroll
    for (int i = 0; i < 3; ++i) kk[i] = __builtin_amdgcn_raw_buffer_load_b32(rk, tid * 4 + i * 1280, rh, 0);
#pragma unroll
    for (int i = 0; i < 8; ++i) kk[3 + i] = __builtin_amdgcn_raw_buffer_load_b32(rk, tid * 4 + (i % 3) * 1280, r0 + (i / 3) * 3840, 0);
#pragma unroll
    for (int i = 0; i < 8; ++i) vv[i] = __builtin_amdgcn_raw_buffer_load_b32(rv, tid * 4 + (i % 3) * 1280, r0 + (i / 3) * 3840, 0);
    const u32x4 shv = *(const u32x4*)(shiftk + 8 * tid);
    Dp = *(const u32*)(ssmD + 2 * tid);
    wrp = *(const u32*)(wr + 2 * tid);
    const u32x2 e = LD64(OFF_E);
    __builtin_amdgcn_sched_barrier(0);
    if (j == 0) { kk[0] = 0; kk[1] = 0; kk[2] = 0; }
    const f32x2 s0 = {lo16(shv.x), lo16(shv.z)}, s1 = {hi16(shv.x), hi16(shv.z)}, s2 = {lo16(shv.y), lo16(shv.w)}, s3 = {hi16(shv.y), hi16(shv.w)};
    f32x2 kf[11], kv[8];
#pragma unroll
    for (int i = 0; i < 11; ++i) kf[i] = f32x2{lo16(kk[i]), hi16(kk[i])};
#pragma unroll
    for (int t = 0; t < 8; ++t) {
      f32x2 a = kf[t] * s3;
      a = kf[t + 1] * s2 + a;
      a = kf[t + 2] * s1 + a;
      a = kf[t + 3] * s0 + a;
      kv[t] = a * f32x2{lo16(vv[t]), hi16(vv[t])};
    }
#pragma unroll
    for (int d = 0; d < 4; ++d) {
      kvp[0][d] = pkbf(kv[7 - 2 * d].x, kv[6 - 2 * d].x);
      kvp[1][d] = pkbf(kv[7 - 2 * d].y, kv[6 - 2 * d].y);
    }
    const u32 xo = OFF_XT + (tid >> 2) * 32768 + j * 128 + (tid & 3) * 16;
    ST128((u32x4{kvp[0][0], kvp[0][1], kvp[0][2], kvp[0][3]}), xo);
    ST128((u32x4{kvp[1][0], kvp[1][1], kvp[1][2], kvp[1][3]}), xo + 64);
    STAMP(2);
    MKTAG(e);
    DRAIN();
    if (lane == 0) ST64((u32x2{tl, th}), OFF_FLAG + wave * 4096 + j * 16);
    STAMP(3);
  } else if (wave < 7) {
    const u32x2 e = LD64(OFF_E);
    if (is_conv) {
      u32x4 kw[8];
      u32x2 kx[8];
      const __amdgpu_buffer_rsrc_t rK = mkrs(ssmk, S_ * D_ * 2);
      const int e0 = (8 * (grp >> 1) + 4 * (wave - 5) + (grp & 1)) * 4096 + lane * 16;
#pragma unroll
      for (int i = 0; i < 8; ++i) {
        kw[i] = __builtin_amdgcn_raw_buffer_load_b128(rK, e0 + (i >> 2) * 8192 + (i & 3) * 1024, 0, 0);
        kx[i] = __builtin_amdgcn_raw_buffer_load_b64(rK, e0 + (i >> 2) * 8192 + (i & 3) * 1024 + 16, 0, 0);
      }
      __builtin_amdgcn_sched_barrier(0);
#pragma unroll
      for (int i = 0; i < 8; ++i) {
        const int c = (wave - 5) * 2 + (i >> 2), a = (i & 3) * 64 + lane;
        const u32 w0 = kw[i].x, w1 = kw[i].y, w2 = kw[i].z, w3 = kw[i].w, w4 = kx[i].x, w5 = kx[i].y;
        const u32 a0 = __builtin_amdgcn_alignbyte(w1, w0, 2), a1 = __builtin_amdgcn_alignbyte(w2, w1, 2),
                  a2 = __builtin_amdgcn_alignbyte(w3, w2, 2), a3 = __builtin_amdgcn_alignbyte(w4, w3, 2),
                  a4 = __builtin_amdgcn_alignbyte(w5, w4, 2);
        const u32 base = c * 4 * KC_STRIDE + (a + 2) * 16;
        if (i == 0) STAMP(2);
        LDS128(base) = kw[i];
        LDS128(base + KC_STRIDE) = u32x4{a0, a1, a2, a3};
        LDS128(base + 2 * KC_STRIDE) = u32x4{w1, w2, w3, w4};
        LDS128(base + 3 * KC_STRIDE) = u32x4{a1, a2, a3, a4};
        if ((i & 3) == 0 && lane == 0) {
          LDS128(base - 16 + KC_STRIDE) = u32x4{0, 0, 0, w0 << 16};
          LDS128(base - 16 + 2 * KC_STRIDE) = u32x4{0, 0, 0, w0};
          LDS128(base - 16 + 3 * KC_STRIDE) = u32x4{0, 0, w0 << 16, a0};
        }
      }
      STAMP(3);
    }
    MKTAG(e);
  } else {
    const u32x2 e = LD64(OFF_E);
    if (lane == 0) ST64((u32x2{SENT, SENT}), OFF_SCORE + j * 16);
    if (lane == 0) ST32((u32)tstart, 110592 + j * 4);
    if (lane < 8) {
      if (is_md) ST128(SENT4, OFF_MOD + md_id * 128 + lane * 16);
      if (is_gu) ST128(SENT4, OFF_A + gu_id * 128 + lane * 16);
    }
    if (is_conv) {
      const u32x4 z = {0, 0, 0, 0};
      if (lane < 60) {
        LDS128(XL + (lane / 30) * XL_STRIDE + (256 + lane % 30) * 16) = z;
        LDS128(XL + (lane / 30 + 2) * XL_STRIDE + (256 + lane % 30) * 16) = z;
      }
      if (lane < 20) LDS128(lane < 16 ? lane * KC_STRIDE : (lane - 16) * 4 * KC_STRIDE + 16) = z;
    }
    MKTAG(e);
    DRAIN();
    if (lane == 0) ST64((u32x2{tl, th}), OFF_F5 + j * 16);
    STAMP(3);
  }
  BAR();
  STAMP(4);

  u32 mp[6] = {SENT, SENT, SENT, SENT, SENT, SENT};
  u32x4 y0 = SENT4, y1 = SENT4;
  if (is_conv) {
    const int c = wave & 3, hf = wave >> 2;
    u32x4 A[8][4];
    const u32 aaddr = (u32)(uintptr_t)smem + (c * 4 + ((li + 1) & 3)) * KC_STRIDE + 16 * lg + 8 * ((li + 1) >> 2) + 256 * hf;
    load_afrags<0>(aaddr, A); load_afrags<2>(aaddr, A); load_afrags<4>(aaddr, A); load_afrags<6>(aaddr, A);
    STAMP(5);
    {
      const u32 fo = (lane < 32 ? OFF_FLAG + (grp >> 5) * 4096 : OFF_F5) + (wave * 32 + (lane & 31)) * 16;
      if (DLY_X) __builtin_amdgcn_s_sleep(DLY_X);
      _Pragma("unroll 1") for (u32 spins = 0; spins < SPIN_CAP; ++spins) {
        CFENCE();
        const u32x2 f = LD64(fo);
        if (TAGOK(f)) break;
        __builtin_amdgcn_s_sleep(2);
      }
      CFENCE();
      STAMP(6);
      const u32 xo = OFF_XT + (grp >> 1) * 32768 + (grp & 1) * 64 + (wave * 32 + (lane >> 2)) * 128 + (lane & 3) * 16;
      const u32x4 x0 = LD128(xo), x1 = LD128(xo + 2048);
      const u32 xa = XL + (lane & 3) * XL_STRIDE + (255 - 32 * wave - (lane >> 2)) * 16;
      LDS128(xa) = x0;
      LDS128(xa - 256) = x1;
    }
    BAR();
    STAMP(7);
    if (wave < 5) { CLEARS(); LOADQ(); }
    const u32 f6o = OFF_F6 + (grp >> 5) * 4096 + ((lane & 16) * 8 + wave * 16 + li) * 16;
    u32x2 f6;
    const u32 bbase = XL + c * XL_STRIDE + (30 + 16 * hf - 2 * li + lg) * 16;
    const u32 pw = hf * 32768 + (lg & 1) * 2048 + (c * 32 + ((2 * li + (lg >> 1) + 4 * c) & 31)) * 16;
    const u32 pr = (tid >> 7) * 4096 + ((tid & 3) * 32 + ((((tid >> 2) & 31) + 4 * (tid & 3)) & 31)) * 16;
    const u32 yo = OFF_YB + (tid >> 3) * 20480 + grp * 128 + (tid & 7) * 16;
    f32x4 acc[8];
#pragma unroll
    for (int n = 0; n < 8; ++n) acc[n] = f32x4{0.f, 0.f, 0.f, 0.f};
    u32x4 B[2][4];
#pragma unroll
    for (int r = 0; r < 4; ++r) B[0][r] = LDS128(bbase + 7 * 512 + 64 * r);
#pragma unroll
    for (int cc = 0; cc < 8; ++cc) {
      if (cc < 7) {
#pragma unroll
        for (int r = 0; r < 4; ++r) B[(cc + 1) & 1][r] = LDS128(bbase + (6 - cc) * 512 + 64 * r);
      }
      __builtin_amdgcn_sched_barrier(0);
#pragma unroll
      for (int r = 0; r < 4; ++r)
#pragma unroll
        for (int n = cc; n < 8; ++n) acc[n] = mfma16(A[n - cc][r], B[cc & 1][r], acc[n]);
      __builtin_amdgcn_sched_barrier(0);
      LDSF4(pw + cc * 4096) = acc[cc];
      if (cc == 0 && wave < 5) {
        asm volatile("s_waitcnt vmcnt(0)" : "+v"(qd[0]), "+v"(qd[1]), "+v"(qd[2]), "+v"(qd[3]), "+v"(qd[4]), "+v"(qd[5]), "+v"(qd[6]), "+v"(qd[7])::"memory");
        if (lane == 0) ST64((u32x2{tl, th}), OFF_F6 + wave * 4096 + j * 16);
      }
      if (cc == 2) f6 = LD64(f6o);
      if (cc == 4) {
        _Pragma("unroll 1") for (u32 spins = 0; spins < SPIN_CAP; ++spins) {
          if (TAGOK(f6)) break;
          __builtin_amdgcn_s_sleep(2);
          CFENCE();
          f6 = LD64(f6o);
        }
      }
      if (cc == 5 && wave < 5) {
#pragma unroll
        for (int i = 0; i < 6; ++i) mp[i] = LD32(OFF_MOD + i * 1280 + tid * 4);
      }
      if (cc == 7 && wave < 5) { y0 = LD128(yin); y1 = LD128(yin + 128); }
      if (cc == 4 || cc == 7) {
        BAR();
        if (cc == 4 && tid == 448) ST64((u32x2{tl, th}), OFF_P + grp * 128);
        const u32 p0 = pr + (cc == 7 ? 16384 : 0);
        const f32x4 lo = LDSF4(p0) + LDSF4(p0 + 32768), hi = LDSF4(p0 + 2048) + LDSF4(p0 + 32768 + 2048);
        ST128((u32x4{san(pkbf(lo.x, lo.y)), san(pkbf(lo.z, lo.w)), san(pkbf(hi.x, hi.y)), san(pkbf(hi.z, hi.w))}),
              yo + (cc == 7 ? 64 * 20480 : 0));
      }
    }
    STAMP(8);
  } else {
    if (wave < 5) {
      __builtin_amdgcn_s_sleep(DLY_C);
      CLEARS();
      LOADQ();
      const u32 fo = OFF_F5 + (tid & 255) * 16;
      _Pragma("unroll 1") for (u32 spins = 0; spins < SPIN_CAP; ++spins) {
        CFENCE();
        const u32x2 f = LD64(fo);
        if (TAGOK(f)) break;
        __builtin_amdgcn_s_sleep(4);
      }
      DRAIN();
      if (lane == 0) ST64((u32x2{tl, th}), OFF_F6 + wave * 4096 + j * 16);
      STAMP(6);
    } else if (is_md) {
      __builtin_amdgcn_s_sleep(DLY_W);
      const int wv = wave - 5;
      const int G = wv * 8 + (lane >> 3);
      const __amdgpu_buffer_rsrc_t rm = mkrs(wmod, 640 * 3840 * 2), rc = mkrs(cvec, 1280);
      u32 cv[4];
#pragma unroll
      for (int u = 0; u < 4; ++u) cv[u] = __builtin_amdgcn_raw_buffer_load_b16(rc, (G * 27 + (lane & 7) + 8 * u) * 2, 0, 0);
      u32x4 wm[27];
      const u32 mo = G * 27 * 7680 + md_id * 128 + (lane & 7) * 16;
#pragma unroll
      for (int i = 0; i < 27; ++i) wm[i] = __builtin_amdgcn_raw_buffer_load_b128(rm, (i >= 19 && G == 23) ? 0x7000000u : mo, i * 7680, 0);
#pragma unroll
      for (int u = 0; u < 4; ++u) {
        const float x = lo16(cv[u]);
        if (u < 3 || (lane & 7) < 4) *(LAS float*)(smem + SIL + (G * 28 + (lane & 7) + 8 * u) * 4) = rbf(x * sigm(x));
      }
      f32x2 a[4];
#pragma unroll
      for (int e = 0; e < 4; ++e) a[e] = f32x2{0.f, 0.f};
#pragma unroll
      for (int i4 = 0; i4 < 7; ++i4) {
        const f32x4 s4 = LDSF4(SIL + (G * 28 + 4 * i4) * 4);
#pragma unroll
        for (int u = 0; u < 4; ++u)
          if (4 * i4 + u < 27) {
#pragma unroll
            for (int e = 0; e < 4; ++e) a[e] += f32x2{lo16(wm[4 * i4 + u][e]), hi16(wm[4 * i4 + u][e])} * s4[u];
          }
      }
      f32x2 y2;
      y2.x = swap16_add(swap32_add(a[0].x, a[2].x), swap32_add(a[1].x, a[3].x));
      y2.y = swap16_add(swap32_add(a[0].y, a[2].y), swap32_add(a[1].y, a[3].y));
      y2.x += dppf<0x128>(y2.x); y2.y += dppf<0x128>(y2.y);
      if ((lane & 8) == 0) *(LAS f32x2*)(smem + RR + wv * 256 + (lane & 7) * 32 + lg * 8) = y2;
      STAMP(6);
    }
    STAMP(7);
  }

  f32x2 qf[8], cb1, sb, gb;
  if (wave < 5) {
    float V[8][2];
#pragma unroll
    for (int t = 0; t < 8; ++t) {
      qf[t] = f32x2{lo16(qd[t]), hi16(qd[t])};
      V[t][0] = qf[t].x + qf[t].y;
      V[t][1] = qf[t].x * qf[t].x + qf[t].y * qf[t].y;
    }
    reduce_tok<2>(V, 0.f, smem, RED1 + wave * 128, lane);
  }
#pragma unroll 1
  for (int pass = 0; pass < 2; ++pass) {
    if (pass == 1) {
      BAR();
      if (is_md && wave == 5) {
        const f32x2 s = *(LAS f32x2*)(smem + RR + (lane & 31) * 8) + *(LAS f32x2*)(smem + RR + 256 + (lane & 31) * 8) +
                        *(LAS f32x2*)(smem + RR + 512 + (lane & 31) * 8);
        if (lane < 32) ST32(san(pkbf(s.x, s.y)), OFF_MOD + md_id * 128 + lane * 4);
      }
    }
    if (wave >= 5 && (pass == 1) == is_md) {
      const u32 lb = (u32)(uintptr_t)smem;
      const int wv = wave - 5;
      if (is_gu) {
        if (pass == 0) __builtin_amdgcn_s_sleep(DLY_W);
        u32 vo = ((lane >> 2) + 16 * wv) * 3456 + gu_id * 64 + (lane & 3) * 16;
        for (int i = wv; i < 40; i += 3, vo += 48 * 3456) { dma16(wg, vo, lb + i * 1024); dma16(wu, vo, lb + UPB + i * 1024); }
      } else if (is_dn) {
        u32 vo = ((lane >> 1) + 32 * wv) * 1280 + dn_id * 32 + (lane & 1) * 16;
        for (int i = wv; i < 54; i += 3, vo += 96 * 1280) dma16(wd, vo, lb + i * 1024);
      }
    }
  }
  STAMP(9);
  f32x2 x1[8];
  if (wave < 5) {
    f32x2 s = *(LAS f32x2*)(smem + RED1 + (lane & 7) * 16);
#pragma unroll
    for (int w = 1; w < 5; ++w) s += *(LAS f32x2*)(smem + RED1 + w * 128 + (lane & 7) * 16);
    const float m = s.x * (1.f / D_);
    const float rstd = __builtin_amdgcn_rsqf(fmaxf(s.y * (1.f / D_) - m * m, 0.f) + 1e-5f);
    _Pragma("unroll 1") for (u32 spins = 0; spins < SPIN_CAP; ++spins) {
      if (__all(umax2(umax2(umax2(mp[0], mp[1]), umax2(mp[2], mp[3])), umax2(mp[4], mp[5])) != SENT)) break;
      if (spins) __builtin_amdgcn_s_sleep(8);
      CFENCE();
#pragma unroll
      for (int i = 0; i < 6; ++i) mp[i] = LD32(OFF_MOD + i * 1280 + tid * 4);
    }
    CFENCE();
    const f32x2 sa = {lo16(mp[0]), hi16(mp[0])}, ga = {lo16(mp[2]), hi16(mp[2])};
    f32x2 ca1;
    { const u32 t = pkbf(1.f + lo16(mp[1]), 1.f + hi16(mp[1])); ca1 = f32x2{lo16(t), hi16(t)}; }
    sb = f32x2{lo16(mp[3]), hi16(mp[3])};
    { const u32 t = pkbf(1.f + lo16(mp[4]), 1.f + hi16(mp[4])); cb1 = f32x2{lo16(t), hi16(t)}; }
    gb = f32x2{lo16(mp[5]), hi16(mp[5])};
    const f32x2 wrf = {lo16(wrp), hi16(wrp)}, gw = cb1 * wrf, Df = {lo16(Dp), hi16(Dp)};
    f32x2 hg[8], kd[8];
#pragma unroll
    for (int t = 0; t < 8; ++t) {
      const float mt = rdl(m, t), rt = rdl(rstd, t);
      hg[t] = ((qf[t] - mt) * rt * ca1 + sa) * ga;
      const u32 u0 = kvp[0][(7 - t) >> 1], u1 = kvp[1][(7 - t) >> 1];
      kd[t] = ((t & 1) ? f32x2{lo16(u0), lo16(u1)} : f32x2{hi16(u0), hi16(u1)}) * Df * hg[t] + qf[t];
    }
    float ex = swap32_add(gw.x + gw.y, sb.x * wrf.x + sb.y * wrf.y);
    ex = swap16_add(ex, ex);
    asm volatile("" : "+v"(ex));
    STAMP(10);
    if (!is_conv) {
      const u32 po = OFF_P + ((j - 160) * 5 / 3) * 128;
      _Pragma("unroll 1") for (u32 spins = 0; spins < SPIN_CAP; ++spins) {
        CFENCE();
        const u32x2 f = LD64(po);
        if (TAGOK(f)) break;
        __builtin_amdgcn_s_sleep(4);
      }
      __builtin_amdgcn_s_sleep(DLY_Y);
    }
    _Pragma("unroll 1") for (u32 spins = 0; spins < SPIN_CAP; ++spins) {
      if (__all(umax2(umax4(y0), umax4(y1)) != SENT)) break;
      if (spins) __builtin_amdgcn_s_sleep(2);
      CFENCE();
      y0 = LD128(yin); y1 = LD128(yin + 128);
    }
    DRAIN();
    STAMP(11);
    float V[8][3];
#pragma unroll
    for (int t = 0; t < 8; ++t) {
      const u32 u0 = y0[t >> 1], u1 = y1[t >> 1];
      x1[t] = hg[t] * ((t & 1) ? f32x2{hi16(u0), hi16(u1)} : f32x2{lo16(u0), lo16(u1)}) + kd[t];
      V[t][0] = x1[t].x + x1[t].y;
      V[t][1] = x1[t].x * x1[t].x + x1[t].y * x1[t].y;
      V[t][2] = x1[t].x * gw.x + x1[t].y * gw.y;
    }
    reduce_tok<3>(V, ex, smem, RED2 + wave * 128, lane);
  } else {
    DRAIN();
  }
  BAR();
  STAMP(12);
  const __amdgpu_buffer_rsrc_t ro = mkrs(out, S_ * D_ * 2);
  u32 w0 = 0, w1 = 0;
  u32 ov = 0, gv = 0;
  if (wave < 6) {
    const f32x4 s = gather5(smem, RED2, lane);
    const float Gs = rdl(s.w, 0), c0s = rdl(s.w, 4);
    const float m = s.x * (1.f / D_);
    const float rstd = __builtin_amdgcn_rsqf(fmaxf(s.y * (1.f / D_) - m * m, 0.f) + 1e-5f);
    const float r = rstd * (s.z - m * Gs) + c0s;
    u32 ob = __float_as_uint(r);
    ob ^= (u32)((int)ob >> 31) | 0x80000000u;
    ob = ob < 0xFFFFE000u ? ob : 0xFFFFE000u;
    const u32 key = (ob & 0xFFFFF000u) | ((2047u - (8 * j + (lane & 7))) << 1);
    u32 mx = umax2(key, dppu<0x124>(key)); mx = umax2(mx, dppu<0x122>(mx)); mx = umax2(mx, dppu<0x121>(mx));
    u32 k2 = key == mx ? 0u : key;
    k2 = umax2(k2, dppu<0x124>(k2)); k2 = umax2(k2, dppu<0x122>(k2)); k2 = umax2(k2, dppu<0x121>(k2));
    const u32 kA = RFL(mx), kB = RFL(k2) | 1u;
    if (wave == 5) {
      if (lane == 0) ST64((u32x2{kA, kB}), OFF_SCORE + j * 16);
      if (is_gu || is_dn) {
        __builtin_amdgcn_s_sleep(DLY_SC);
        u32x2 g[4];
        _Pragma("unroll 1") for (u32 spins = 0; spins < SPIN_CAP; ++spins) {
          CFENCE();
#pragma unroll
          for (int i = 0; i < 4; ++i) g[i] = LD64(OFF_SCORE + lane * 16 + i * 1024);
          if (__all(umax2(umax2(umax2(g[0].x, g[0].y), umax2(g[1].x, g[1].y)), umax2(umax2(g[2].x, g[2].y), umax2(g[3].x, g[3].y))) != SENT)) break;
          __builtin_amdgcn_s_sleep(2);
        }
        CFENCE();
        STAMP(13);
        w0 = RFL(wave_umax(umax2(umax2(g[0].x, g[1].x), umax2(g[2].x, g[3].x))));
        u32 c2 = 0;
#pragma unroll
        for (int i = 0; i < 4; ++i) c2 = umax2(c2, g[i].x == w0 ? g[i].y : g[i].x);
        w1 = RFL(wave_umax(c2));
        const u32 wk = lane < 32 ? w0 : w1;
        const u32 tok = 2047 - ((wk >> 1) & 2047);
        if (is_gu) {
          const u32 l5 = lane & 31, o2 = 1024 + (l5 & 15) * 16;
          const u32 row = OFF_CAND + (tok >> 3) * 2560 + (wk & 1) * 1280;
          u32x4 h0, h1, h2;
          _Pragma("unroll 1") for (u32 spins = 0; spins < SPIN_CAP; ++spins) {
            CFENCE();
            h0 = LD128(row + l5 * 16); h1 = LD128(row + 512 + l5 * 16); h2 = LD128(row + o2);
            if (__all(umax2(umax2(umax4(h0), umax4(h1)), umax4(h2)) != SENT)) break;
            __builtin_amdgcn_s_sleep(1);
          }
          CFENCE();
          const u32 hb = HR + (lane >> 5) * HR_STRIDE;
          LDS128(hb + l5 * 16) = h0; LDS128(hb + 512 + l5 * 16) = h1; LDS128(hb + o2) = h2;
        } else {
          if (dn_id == 0 && lane == 0) ST64((u32x2{tl, th}), OFF_E);
          _Pragma("unroll 1") for (u32 spins = 0; spins < SPIN_CAP; ++spins) {
            CFENCE();
            const u32x2 f = LD64(OFF_RD + (tok >> 3) * 64 + (dn_id >> 3) * 8);
            if (TAGOK(f)) break;
            __builtin_amdgcn_s_sleep(1);
          }
          CFENCE();
          ov = __builtin_amdgcn_raw_buffer_load_b16(ro, tok * 1280 + dn_id * 32 + li * 2, 0, 16);
          gv = __builtin_amdgcn_raw_buffer_load_b16(rs, OFF_MOD + 5 * 1280 + dn_id * 32 + li * 2, 0, 16);
        }
      }
    } else {
      const int tA = (2047 - ((kA >> 1) & 2047)) & 7, tB = (2047 - ((kB >> 1) & 2047)) & 7;
      u32 d0 = 0, d1 = 0, op[8];
#pragma unroll
      for (int t = 0; t < 8; ++t) {
        const float mt = rdl(m, t), rt = rdl(rstd, t);
        const f32x2 h2 = (x1[t] - mt) * rt * cb1 + sb;
        const f32x2 o = gb * h2 + x1[t];
        op[t] = pkbf(o.x, o.y);
        const u32 hp = pkbf(h2.x, h2.y);
        d0 = t == tA ? hp : d0; d1 = t == tB ? hp : d1;
      }
      ST32(san(d0), OFF_CAND + j * 2560 + tid * 4);
      ST32(san(d1), OFF_CAND + j * 2560 + 1280 + tid * 4);
#pragma unroll
      for (int t = 0; t < 8; ++t) {
        if (OUT_AUX == 16 || t == tA || t == tB) __builtin_amdgcn_raw_buffer_store_b32(op[t], ro, tid * 4 + (t % 3) * 1280, j * 10240 + (t / 3) * 3840, 16);
        else __builtin_amdgcn_raw_buffer_store_b32(op[t], ro, tid * 4 + (t % 3) * 1280, j * 10240 + (t / 3) * 3840, OUT_AUX);
      }
      if (!is_gu && !is_dn) {
        DRAIN();
        if (lane == 0) ST64((u32x2{tl, th}), OFF_RD + j * 64 + wave * 8);
      }
    }
  }
  if (is_gu) {
    u32x4 Wf[20];
    if (wave < 4) {
      LAS char* p = smem + ((li >> 1) & 1) * UPB + (8 * lg + (li >> 2)) * 64 + wave * 16 + (li & 1) * 8;
#pragma unroll
      for (int s = 0; s < 20; ++s) Wf[s] = tr_frag(p + s * 2048, 256);
    }
    if (wave < 5) {
      DRAIN();
      if (lane == 0) ST64((u32x2{tl, th}), OFF_RD + j * 64 + wave * 8);
    }
    BAR();
    STAMP(14);
    if (wave < 4) {
      f32x4 a0 = {0.f, 0.f, 0.f, 0.f}, a1 = a0;
      const u32 hb = HR + (li & 1) * HR_STRIDE + lg * 16;
#pragma unroll
      for (int s = 0; s < 20; s += 2) {
        a0 = mfma16(Wf[s], LDS128(hb + s * 64), a0);
        a1 = mfma16(Wf[s + 1], LDS128(hb + s * 64 + 64), a1);
      }
      a0 += a1;
      float av[4];
#pragma unroll
      for (int e = 0; e < 4; ++e) {
        auto r = __builtin_amdgcn_permlane32_swap(__float_as_uint(a0[e]), __float_as_uint(a0[e]), false, false);
        const float g = rbf(__uint_as_float(r[0])), u = rbf(__uint_as_float(r[1]));
        av[e] = rbf(g * sigm(g)) * u;
      }
      if (li < 2 && lg < 2) ST64((u32x2{san(pkbf(av[0], av[1])), san(pkbf(av[2], av[3]))}), OFF_A + gu_id * 128 + li * 64 + wave * 16 + lg * 8);
    }
    STAMP(15);
  } else if (is_dn) {
    u32x4 Wf[7];
    LAS char* p = smem + (32 * wave + 8 * lg + (li >> 2)) * 32 + (li & 3) * 8;
#pragma unroll
    for (int s = 0; s < 6; ++s) Wf[s] = tr_frag(p + s * 8192, 128);
    Wf[6] = tr_frag(p + (wave < 6 ? 6 * 8192 : 0), 128);
    if (wave < 5) {
      DRAIN();
      if (lane == 0) ST64((u32x2{tl, th}), OFF_RD + j * 64 + wave * 8);
    }
    BAR();
    __builtin_amdgcn_s_sleep(DLY_A);
    u32x4 Bf[7];
    const u32 ao = OFF_A + wave * 128 + (li & 1) * 64 + lg * 16;
    _Pragma("unroll 1") for (u32 spins = 0; spins < SPIN_CAP; ++spins) {
      CFENCE();
      u32 mx = 0;
#pragma unroll
      for (int s = 0; s < 6; ++s) Bf[s] = LD128(ao + s * 1024);
      Bf[6] = LD128(ao + (wave < 6 ? 6 * 1024 : 0));
#pragma unroll
      for (int s = 0; s < 7; ++s) mx = umax2(mx, umax4(Bf[s]));
      if (__all(mx != SENT)) break;
      __builtin_amdgcn_s_sleep(1);
    }
    CFENCE();
    STAMP(14);
    f32x4 a0 = {0.f, 0.f, 0.f, 0.f}, a1 = a0;
#pragma unroll
    for (int s = 0; s < 6; s += 2) { a0 = mfma16(Wf[s], Bf[s], a0); a1 = mfma16(Wf[s + 1], Bf[s + 1], a1); }
    if (wave < 6) a0 = mfma16(Wf[6], Bf[6], a0);
    if (li < 2) LDSF4(RR + ((wave * 2 + li) * 16 + 4 * lg) * 4) = a0 + a1;
    BAR();
    if (wave == 5) {
      float s = 0.f;
#pragma unroll
      for (int w = 0; w < 8; ++w) s += *(LAS float*)(smem + RR + w * 128 + (lane >> 5) * 64 + li * 4);
      const u32 wk = lane < 32 ? w0 : w1, kb = wk & 0xFFFFF000u;
      const float rv = rbf(__uint_as_float((kb & 0x80000000u) ? (kb ^ 0x80000000u) : ~kb));
      const float y = rbf(rbf(s) * rbf(sigm(rv)));
      const float o = lo16(ov) + lo16(gv) * y;
      if ((lane & 16) == 0) __builtin_amdgcn_raw_buffer_store_b16((u16)pkbf(o, o), ro, (2047 - ((wk >> 1) & 2047)) * 1280 + dn_id * 32 + li * 2, 0, 16);
    }
    STAMP(15);
  }
  {
    const u32x4 ts = LD128(110592 + lane * 16);
    const u32 me = (u32)tstart;
    u32 d = umax2(umax2(me - ts.x + 100000u, me - ts.y + 100000u), umax2(me - ts.z + 100000u, me - ts.w + 100000u));
    d = RFL(wave_umax(d)) - 100000u;
    for (int i = 0; i < 100000; ++i) { if ((u32)__builtin_amdgcn_s_memrealtime() - me + d >= 4000) break; __builtin_amdgcn_s_sleep(1); }
  }
}

extern "C" void kernel_launch(void* const* d_in, const int* in_sizes, int n_in, void* d_out, int out_size,
                              void* d_ws, size_t ws_size, hipStream_t stream) {
  (void)in_sizes; (void)n_in; (void)out_size; (void)ws_size;
  (void)hipFuncSetAttribute((const void*)fused_block, hipFuncAttributeMaxDynamicSharedMemorySize, (int)LDS_BYTES);
  fused_block<<<256, 512, LDS_BYTES, stream>>>(
      (const u16*)d_in[0], (const u16*)d_in[1], (const u16*)d_in[2], (const u16*)d_in[3], (const u16*)d_in[4],
      (const u16*)d_in[5], (const u16*)d_in[7], (const u16*)d_in[8], (const u16*)d_in[9], (const u16*)d_in[10],
      (const u16*)d_in[11], (const u16*)d_in[12], (u16*)d_out, (char*)d_ws);
}
```

```cpp
#include <hip/hip_runtime.h>
#include <hip/hip_bf16.h>
#include <stdint.h>

typedef unsigned short u16;
typedef unsigned int u32;
typedef unsigned long long u64;
typedef u32 u32x2 __attribute__((ext_vector_type(2)));
typedef u32 u32x4 __attribute__((ext_vector_type(4)));
typedef float f32x2 __attribute__((ext_vector_type(2)));
typedef float f32x4 __attribute__((ext_vector_type(4)));
typedef short s16x4 __attribute__((ext_vector_type(4)));
typedef __bf16 bf16x2 __attribute__((ext_vector_type(2)));
typedef __bf16 bf16x8 __attribute__((ext_vector_type(8)));
#define LAS __attribute__((address_space(3)))
#define GAS __attribute__((address_space(1)))
#define RLX_AGENT __ATOMIC_RELAXED, __HIP_MEMORY_SCOPE_AGENT
#define DI __device__ __forceinline__

constexpr int S_ = 2048, D_ = 640, F_ = 1728;
constexpr u32 SENT = 0xFFFFFFFFu;
constexpr u32 SPIN_CAP = 1u << 16;
constexpr u32 OFF_E = 0, OFF_FLAG = 4096, OFF_F5 = OFF_FLAG + 5 * 4096, OFF_F6 = OFF_F5 + 4096, OFF_SCORE = OFF_F6 + 5 * 4096,
              OFF_MOD = OFF_SCORE + 4096, OFF_A = OFF_MOD + 8192, OFF_RD = OFF_A + 8192, OFF_P = OFF_RD + 16384;
constexpr u32 OFF_XT = 131072, OFF_YB = OFF_XT + 2621440, OFF_CAND = OFF_YB + 2621440, OFF_END = OFF_CAND + 655360;
constexpr u32 OFF_DBG = 8u << 20;
constexpr u32 KC_STRIDE = 4160, XL = 66560, XL_STRIDE = 4640, UPB = 40960 + 32, RED1 = 86016, RED2 = RED1 + 640, RR = RED2 + 640,
              TAGW = RR + 1024, WINW = TAGW + 16, SIL = WINW + 16, HR = SIL + 3072, HR_STRIDE = 1344, LDS_BYTES = HR + 2 * HR_STRIDE;
#define DLY_Y 10
#define DLY_SC 10
#define DLY_A 16
#define DLY_W 16
#define DLY_C 24
#define DLY_X 0
#define MOD_CC 4
#define OUT_AUX 0

#define STAMP(i) do {} while (0)

DI float lo16(u32 x) { return __uint_as_float(x << 16); }
DI float hi16(u32 x) { return __uint_as_float(x & 0xffff0000u); }
DI u32 pkbf(float lo, float hi) { f32x2 f = {lo, hi}; return __builtin_bit_cast(u32, __builtin_convertvector(f, bf16x2)); }
DI float rbf(float x) { return lo16(pkbf(x, x)); }
DI u32 san(u32 x) { return x < 0xFFFFFFFEu ? x : 0xFFFFFFFEu; }
DI u32 umax2(u32 a, u32 b) { return a > b ? a : b; }
DI u32 umax4(u32x4 v) { return umax2(umax2(v.x, v.y), umax2(v.z, v.w)); }
DI float sigm(float x) { return __builtin_amdgcn_rcpf(1.f + __expf(-x)); }
template <int CTRL> DI u32 dppu(u32 x) { return (u32)__builtin_amdgcn_update_dpp(0, (int)x, CTRL, 0xf, 0xf, false); }
template <int CTRL> DI float dppf(float x) { return __uint_as_float(dppu<CTRL>(__float_as_uint(x))); }
DI float swap32_add(float a, float b) { auto r = __builtin_amdgcn_permlane32_swap(__float_as_uint(a), __float_as_uint(b), false, false); return __uint_as_float(r[0]) + __uint_as_float(r[1]); }
DI float swap16_add(float a, float b) { auto r = __builtin_amdgcn_permlane16_swap(__float_as_uint(a), __float_as_uint(b), false, false); return __uint_as_float(r[0]) + __uint_as_float(r[1]); }
DI float row_sum(float x) { x += dppf<0x128>(x); x += dppf<0x124>(x); x += dppf<0x122>(x); x += dppf<0x121>(x); return x; }
DI u32 wave_umax(u32 x) {
  x = umax2(x, dppu<0x128>(x)); x = umax2(x, dppu<0x124>(x)); x = umax2(x, dppu<0x122>(x)); x = umax2(x, dppu<0x121>(x));
  auto r = __builtin_amdgcn_permlane16_swap(x, x, false, false); x = umax2(r[0], r[1]);
  auto s = __builtin_amdgcn_permlane32_swap(x, x, false, false); return umax2(s[0], s[1]);
}
DI f32x4 mfma16(u32x4 a, u32x4 b, f32x4 c) { return __builtin_amdgcn_mfma_f32_16x16x32_bf16(__builtin_bit_cast(bf16x8, a), __builtin_bit_cast(bf16x8, b), c, 0, 0, 0); }
#define LDS128(off) (*(LAS u32x4*)(smem + (off)))
#define LDSF4(off) (*(LAS f32x4*)(smem + (off)))
#define LDS32(off) (*(LAS u32*)(smem + (off)))
#define RFL(x) ((u32)__builtin_amdgcn_readfirstlane((int)(x)))
#define CFENCE() asm volatile("" ::: "memory")

#define RS_STEP(n) "v_add_f32_dpp %0, %0, %0 row_ror:" #n " row_mask:0xf bank_mask:0xf\n\tv_add_f32_dpp %1, %1, %1 row_ror:" #n " row_mask:0xf bank_mask:0xf\n\t" \
                   "v_add_f32_dpp %2, %2, %2 row_ror:" #n " row_mask:0xf bank_mask:0xf\n\tv_add_f32_dpp %3, %3, %3 row_ror:" #n " row_mask:0xf bank_mask:0xf\n\t"
#define RS_STEP3(n) "v_add_f32_dpp %4, %4, %4 row_ror:" #n " row_mask:0xf bank_mask:0xf\n\tv_add_f32_dpp %5, %5, %5 row_ror:" #n " row_mask:0xf bank_mask:0xf\n\t" \
                    "v_add_f32_dpp %6, %6, %6 row_ror:" #n " row_mask:0xf bank_mask:0xf\n\t"
template <int NQ>
DI void reduce_tok(const float (&V)[8][NQ], float ex, LAS char* smem, u32 dst, int lane) {
  float W[4][NQ], X[2][NQ];
#pragma unroll
  for (int t = 0; t < 4; ++t)
#pragma unroll
    for (int q = 0; q < NQ; ++q) W[t][q] = swap32_add(V[t][q], V[t + 4][q]);
#pragma unroll
  for (int t = 0; t < 2; ++t)
#pragma unroll
    for (int q = 0; q < NQ; ++q) X[t][q] = swap16_add(W[t][q], W[t + 2][q]);
  const int sel = lane & 15;
  const u32 ad = dst + (sel + 2 * (lane >> 4)) * 16;
  if constexpr (NQ == 2) {
    asm volatile("s_nop 1\n\t" RS_STEP(8) RS_STEP(4) RS_STEP(2) RS_STEP(1) "s_nop 0"
                 : "+v"(X[0][0]), "+v"(X[0][1]), "+v"(X[1][0]), "+v"(X[1][1]));
    if (sel < 2) *(LAS f32x2*)(smem + ad) = f32x2{sel ? X[1][0] : X[0][0], sel ? X[1][1] : X[0][1]};
  } else {
    asm volatile("s_nop 1\n\t" RS_STEP(8) RS_STEP3(8) RS_STEP(4) RS_STEP3(4) RS_STEP(2) RS_STEP3(2) RS_STEP(1) RS_STEP3(1) "s_nop 0"
                 : "+v"(X[0][0]), "+v"(X[0][1]), "+v"(X[1][0]), "+v"(X[1][1]), "+v"(X[0][2]), "+v"(X[1][2]), "+v"(ex));
    if (sel < 2) LDSF4(ad) = f32x4{sel ? X[1][0] : X[0][0], sel ? X[1][1] : X[0][1], sel ? X[1][2] : X[0][2], ex};
  }
}
DI f32x4 gather5(LAS char* smem, u32 base, int lane) {
  f32x4 s = LDSF4(base + (lane & 7) * 16);
#pragma unroll
  for (int w = 1; w < 5; ++w) s += LDSF4(base + w * 128 + (lane & 7) * 16);
  return s;
}
DI float rdl(float x, int l) { return __uint_as_float((u32)__builtin_amdgcn_readlane((int)__float_as_uint(x), l)); }

template <int M>
DI void load_afrags(u32 addr, u32x4 (&A)[8][4]) {
  u32x2 o[16];
  asm volatile(
      "ds_read_b64 %0, %16 offset:%17\n\tds_read_b64 %1, %16 offset:%17+8\n\t"
      "ds_read_b64 %2, %16 offset:%17+64\n\tds_read_b64 %3, %16 offset:%17+72\n\t"
      "ds_read_b64 %4, %16 offset:%17+128\n\tds_read_b64 %5, %16 offset:%17+136\n\t"
      "ds_read_b64 %6, %16 offset:%17+192\n\tds_read_b64 %7, %16 offset:%17+200\n\t"
      "ds_read_b64 %8, %16 offset:%17+512\n\tds_read_b64 %9, %16 offset:%17+520\n\t"
      "ds_read_b64 %10, %16 offset:%17+576\n\tds_read_b64 %11, %16 offset:%17+584\n\t"
      "ds_read_b64 %12, %16 offset:%17+640\n\tds_read_b64 %13, %16 offset:%17+648\n\t"
      "ds_read_b64 %14, %16 offset:%17+704\n\tds_read_b64 %15, %16 offset:%17+712\n\t"
      "s_waitcnt lgkmcnt(0)"
      : "=&v"(o[0]), "=&v"(o[1]), "=&v"(o[2]), "=&v"(o[3]), "=&v"(o[4]), "=&v"(o[5]), "=&v"(o[6]), "=&v"(o[7]),
        "=&v"(o[8]), "=&v"(o[9]), "=&v"(o[10]), "=&v"(o[11]), "=&v"(o[12]), "=&v"(o[13]), "=&v"(o[14]), "=&v"(o[15])
      : "v"(addr), "i"(M * 512)
      : "memory");
#pragma unroll
  for (int r = 0; r < 4; ++r) {
    A[M][r] = u32x4{o[2 * r].x, o[2 * r].y, o[2 * r + 1].x, o[2 * r + 1].y};
    A[M + 1][r] = u32x4{o[8 + 2 * r].x, o[8 + 2 * r].y, o[8 + 2 * r + 1].x, o[8 + 2 * r + 1].y};
  }
}
DI u32x4 tr_frag(LAS char* p, u32 step) {
  typedef short v4i16_t __attribute__((ext_vector_type(4)));
  u32x2 a = __builtin_bit_cast(u32x2, __builtin_amdgcn_ds_read_tr16_b64_v4i16((LAS v4i16_t*)p));
  u32x2 b = __builtin_bit_cast(u32x2, __builtin_amdgcn_ds_read_tr16_b64_v4i16((LAS v4i16_t*)(p + step)));
  return u32x4{a.x, a.y, b.x, b.y};
}

DI void dma16(const void* base, u32 voff, u32 lds) {
  u32 keep;
  asm volatile("s_mov_b32 %0, m0\n\ts_mov_b32 m0, %3\n\ts_nop 0\n\tglobal_load_lds_dwordx4 %1, %2\n\ts_mov_b32 m0, %0"
               : "=&s"(keep) : "v"(voff), "s"(base), "s"(lds) : "memory");
}
#define BAR() asm volatile("s_waitcnt lgkmcnt(0)\n\ts_barrier" ::: "memory")
#define DRAIN() do { CFENCE(); __builtin_amdgcn_s_waitcnt(0x0F70); CFENCE(); } while (0)
DI __amdgpu_buffer_rsrc_t mkrs(const void* p, u32 bytes) { return __builtin_amdgcn_make_buffer_rsrc((void*)p, (short)0, (int)bytes, 0x00020000); }

__global__ void __launch_bounds__(512, 2)
fused_block(const u16* __restrict__ q, const u16* __restrict__ k, const u16* __restrict__ v,
            const u16* __restrict__ shiftk, const u16* __restrict__ ssmk, const u16* __restrict__ ssmD,
            const u16* __restrict__ wr, const u16* __restrict__ wg, const u16* __restrict__ wu,
            const u16* __restrict__ wd, const u16* __restrict__ cvec, const u16* __restrict__ wmod,
            u16* __restrict__ out, char* __restrict__ ws) {
  extern __shared__ __attribute__((aligned(16))) char smem_g[];
  LAS char* smem = (LAS char*)smem_g;
  STAMP(0);
  asm volatile("" ::"s"(q), "s"(k), "s"(v), "s"(shiftk), "s"(ssmk), "s"(ssmD), "s"(wr), "s"(wg), "s"(wu), "s"(wd),
               "s"(cvec), "s"(wmod), "s"(out), "s"(ws));
  const int tid = threadIdx.x, lane = tid & 63;
  const int wave = __builtin_amdgcn_readfirstlane(tid >> 6);
  const int b = blockIdx.x, xcd = b & 7, slot = b >> 3;
  const bool is_conv = slot < 20;
  const int grp = xcd * 20 + slot;
  const int sa_ = slot - 20;
  const int j = is_conv ? 96 + grp : xcd * 12 + sa_;
  const int gu_id = xcd * 7 + sa_, dn_id = xcd * 5 + sa_ - 7, md_id = xcd + 8 * (11 - sa_);
  const bool is_gu = !is_conv && sa_ < 7 && gu_id < 54;
  const bool is_dn = !is_conv && sa_ >= 7;
  const bool is_md = !is_conv && md_id < 60;
  const __amdgpu_buffer_rsrc_t rs = mkrs(ws, OFF_END);
#define LD128(off) __builtin_amdgcn_raw_buffer_load_b128(rs, (off), 0, 16)
#define LD64(off) __builtin_amdgcn_raw_buffer_load_b64(rs, (off), 0, 16)
#define LD32(off) __builtin_amdgcn_raw_buffer_load_b32(rs, (off), 0, 16)
#define ST128(val, off) __builtin_amdgcn_raw_buffer_store_b128((val), rs, (off), 0, 16)
#define ST64(val, off) __builtin_amdgcn_raw_buffer_store_b64((val), rs, (off), 0, 16)
#define ST32(val, off) __builtin_amdgcn_raw_buffer_store_b32((val), rs, (off), 0, 16)
  const u32x4 SENT4 = {SENT, SENT, SENT, SENT};
  const int li = lane & 15, lg = lane >> 4;
  const u32 yin = OFF_YB + (j >> 1) * 20480 + (j & 1) * 64 + (tid >> 2) * 256 + (tid & 3) * 16;

  u32 Dp = 0, wrp = 0, tl, th;
  u32 kvp[2][4];
  u32 qd[8];
  const __amdgpu_buffer_rsrc_t rq = mkrs(q, S_ * D_ * 2);
#define LOADQ() _Pragma("unroll") for (int t = 0; t < 8; ++t) qd[t] = __builtin_amdgcn_raw_buffer_load_b32(rq, tid * 4 + (t % 3) * 1280, j * 10240 + (t / 3) * 3840, 0)
#define MKTAG(e) do { u32 l_, h_; asm volatile("s_waitcnt vmcnt(0)\n\tv_readfirstlane_b32 %0, %2\n\tv_readfirstlane_b32 %1, %3" : "=s"(l_), "=s"(h_) : "v"((e).x), "v"((e).y) : "memory"); \
    DRAIN(); const u64 t_ = (((u64)h_ << 32) | l_) + 1; tl = (u32)t_; th = (u32)(t_ >> 32); } while (0)
#define CLEARS() do { ST128(SENT4, yin); ST128(SENT4, yin + 128); ST32(SENT, OFF_CAND + j * 2560 + tid * 4); ST32(SENT, OFF_CAND + j * 2560 + 1280 + tid * 4); } while (0)
#define DMA_GU() do { u32 vo = ((lane >> 2) + 16 * (wave - 5)) * 3456 + gu_id * 64 + (lane & 3) * 16; const u32 lb = (u32)(uintptr_t)smem; \
    for (int i = wave - 5; i < 40; i += 3, vo += 48 * 3456) { dma16(wg, vo, lb + i * 1024); dma16(wu, vo, lb + UPB + i * 1024); } } while (0)
#define TAGOK(f) (__all((((f).x ^ tl) | ((f).y ^ th)) == 0))
  u32x2 e;
  asm volatile("global_load_dwordx2 %0, %1, %2 sc1" : "=v"(e) : "v"(0u), "s"(ws) : "memory");
  if (wave < 5) {
    u32 kk[11], vv[8];
    const __amdgpu_buffer_rsrc_t rk = mkrs(k, S_ * D_ * 2), rv = mkrs(v, S_ * D_ * 2);
    const int r0 = j * 10240, rh = j ? r0 - 3840 : 0;
#pragma unroll
    for (int i = 0; i < 3; ++i) kk[i] = __builtin_amdgcn_raw_buffer_load_b32(rk, tid * 4 + i * 1280, rh, 0);
#pragma unroll
    for (int i = 0; i < 8; ++i) kk[3 + i] = __builtin_amdgcn_raw_buffer_load_b32(rk, tid * 4 + (i % 3) * 1280, r0 + (i / 3) * 3840, 0);
#pragma unroll
    for (int i = 0; i < 8; ++i) vv[i] = __builtin_amdgcn_raw_buffer_load_b32(rv, tid * 4 + (i % 3) * 1280, r0 + (i / 3) * 3840, 0);
    const u32x4 shv = *(const u32x4*)(shiftk + 8 * tid);
    Dp = *(const u32*)(ssmD + 2 * tid);
    wrp = *(const u32*)(wr + 2 * tid);
    __builtin_amdgcn_sched_barrier(0);
    if (j == 0) { kk[0] = 0; kk[1] = 0; kk[2] = 0; }
    const f32x2 s0 = {lo16(shv.x), lo16(shv.z)}, s1 = {hi16(shv.x), hi16(shv.z)}, s2 = {lo16(shv.y), lo16(shv.w)}, s3 = {hi16(shv.y), hi16(shv.w)};
    f32x2 kf[11], kv[8];
#pragma unroll
    for (int i = 0; i < 11; ++i) kf[i] = f32x2{lo16(kk[i]), hi16(kk[i])};
#pragma unroll
    for (int t = 0; t < 8; ++t) {
      f32x2 a = kf[t] * s3;
      a = kf[t + 1] * s2 + a;
      a = kf[t + 2] * s1 + a;
      a = kf[t + 3] * s0 + a;
      kv[t] = a * f32x2{lo16(vv[t]), hi16(vv[t])};
    }
#pragma unroll
    for (int d = 0; d < 4; ++d) {
      kvp[0][d] = pkbf(kv[7 - 2 * d].x, kv[6 - 2 * d].x);
      kvp[1][d] = pkbf(kv[7 - 2 * d].y, kv[6 - 2 * d].y);
    }
    const u32 xo = OFF_XT + (tid >> 2) * 32768 + j * 128 + (tid & 3) * 16;
    ST128((u32x4{kvp[0][0], kvp[0][1], kvp[0][2], kvp[0][3]}), xo);
    ST128((u32x4{kvp[1][0], kvp[1][1], kvp[1][2], kvp[1][3]}), xo + 64);
    STAMP(2);
    MKTAG(e);
    if (lane == 0) ST64((u32x2{tl, th}), OFF_FLAG + wave * 4096 + j * 16);
    STAMP(3);
  } else if (wave < 7) {
    if (is_conv) {
      u32x4 kw[8];
      u32x2 kx[8];
      const __amdgpu_buffer_rsrc_t rK = mkrs(ssmk, S_ * D_ * 2);
      const int e0 = (8 * (grp >> 1) + 4 * (wave - 5) + (grp & 1)) * 4096 + lane * 16;
#pragma unroll
      for (int i = 0; i < 8; ++i) {
        kw[i] = __builtin_amdgcn_raw_buffer_load_b128(rK, e0 + (i >> 2) * 8192 + (i & 3) * 1024, 0, 0);
        kx[i] = __builtin_amdgcn_raw_buffer_load_b64(rK, e0 + (i >> 2) * 8192 + (i & 3) * 1024 + 16, 0, 0);
      }
      __builtin_amdgcn_sched_barrier(0);
#pragma unroll
      for (int i = 0; i < 8; ++i) {
        const int c = (wave - 5) * 2 + (i >> 2), a = (i & 3) * 64 + lane;
        const u32 w0 = kw[i].x, w1 = kw[i].y, w2 = kw[i].z, w3 = kw[i].w, w4 = kx[i].x, w5 = kx[i].y;
        const u32 a0 = __builtin_amdgcn_alignbyte(w1, w0, 2), a1 = __builtin_amdgcn_alignbyte(w2, w1, 2),
                  a2 = __builtin_amdgcn_alignbyte(w3, w2, 2), a3 = __builtin_amdgcn_alignbyte(w4, w3, 2),
                  a4 = __builtin_amdgcn_alignbyte(w5, w4, 2);
        const u32 base = c * 4 * KC_STRIDE + (a + 2) * 16;
        if (i == 0) STAMP(2);
        LDS128(base) = kw[i];
        LDS128(base + KC_STRIDE) = u32x4{a0, a1, a2, a3};
        LDS128(base + 2 * KC_STRIDE) = u32x4{w1, w2, w3, w4};
        LDS128(base + 3 * KC_STRIDE) = u32x4{a1, a2, a3, a4};
        if ((i & 3) == 0 && lane == 0) {
          LDS128(base - 16 + KC_STRIDE) = u32x4{0, 0, 0, w0 << 16};
          LDS128(base - 16 + 2 * KC_STRIDE) = u32x4{0, 0, 0, w0};
          LDS128(base - 16 + 3 * KC_STRIDE) = u32x4{0, 0, w0 << 16, a0};
        }
      }
      STAMP(3);
    }
    MKTAG(e);
  } else {
    if (lane == 0) ST64((u32x2{SENT, SENT}), OFF_SCORE + j * 16);
    if (lane < 8) {
      if (is_md) ST128(SENT4, OFF_MOD + md_id * 128 + lane * 16);
      if (is_gu) ST128(SENT4, OFF_A + gu_id * 128 + lane * 16);
    }
    if (is_conv) {
      const u32x4 z = {0, 0, 0, 0};
      if (lane < 60) {
        LDS128(XL + (lane / 30) * XL_STRIDE + (256 + lane % 30) * 16) = z;
        LDS128(XL + (lane / 30 + 2) * XL_STRIDE + (256 + lane % 30) * 16) = z;
      }
      if (lane < 20) LDS128(lane < 16 ? lane * KC_STRIDE : (lane - 16) * 4 * KC_STRIDE + 16) = z;
    }
    MKTAG(e);
    if (lane == 0) ST64((u32x2{tl, th}), OFF_F5 + j * 16);
    STAMP(3);
  }
  BAR();
  STAMP(4);

  u32 mp[6] = {SENT, SENT, SENT, SENT, SENT, SENT};
  if (is_conv) {
    const int c = wave & 3, hf = wave >> 2;
    u32x4 A[8][4];
    const u32 aaddr = (u32)(uintptr_t)smem + (c * 4 + ((li + 1) & 3)) * KC_STRIDE + 16 * lg + 8 * ((li + 1) >> 2) + 256 * hf;
    load_afrags<0>(aaddr, A); load_afrags<2>(aaddr, A); load_afrags<4>(aaddr, A); load_afrags<6>(aaddr, A);
    STAMP(5);
    {
      const u32 fo = (lane < 32 ? OFF_FLAG + (grp >> 5) * 4096 : OFF_F5) + (wave * 32 + (lane & 31)) * 16;
      if (DLY_X) __builtin_amdgcn_s_sleep(DLY_X);
      _Pragma("unroll 1") for (u32 spins = 0; spins < SPIN_CAP; ++spins) {
        CFENCE();
        const u32x2 f = LD64(fo);
        if (TAGOK(f)) break;
        __builtin_amdgcn_s_sleep(2);
      }
      CFENCE();
      STAMP(6);
      const u32 xo = OFF_XT + (grp >> 1) * 32768 + (grp & 1) * 64 + (wave * 32 + (lane >> 2)) * 128 + (lane & 3) * 16;
      const u32x4 x0 = LD128(xo), x1 = LD128(xo + 2048);
      const u32 xa = XL + (lane & 3) * XL_STRIDE + (255 - 32 * wave - (lane >> 2)) * 16;
      LDS128(xa) = x0;
      LDS128(xa - 256) = x1;
    }
    BAR();
    STAMP(7);
    if (wave < 5) { CLEARS(); LOADQ(); }
    const u32 f6o = OFF_F6 + (grp >> 5) * 4096 + ((lane & 16) * 8 + wave * 16 + li) * 16;
    u32x2 f6;
    const u32 bbase = XL + c * XL_STRIDE + (30 + 16 * hf - 2 * li + lg) * 16;
    const u32 pw = hf * 32768 + (lg & 1) * 2048 + (c * 32 + ((2 * li + (lg >> 1) + 4 * c) & 31)) * 16;
    const u32 pr = (tid >> 7) * 4096 + ((tid & 3) * 32 + ((((tid >> 2) & 31) + 4 * (tid & 3)) & 31)) * 16;
    const u32 yo = OFF_YB + (tid >> 3) * 20480 + grp * 128 + (tid & 7) * 16;
    f32x4 acc[8];
#pragma unroll
    for (int n = 0; n < 8; ++n) acc[n] = f32x4{0.f, 0.f, 0.f, 0.f};
    u32x4 B[2][4];
#pragma unroll
    for (int r = 0; r < 4; ++r) B[0][r] = LDS128(bbase + 7 * 512 + 64 * r);
#pragma unroll
    for (int cc = 0; cc < 8; ++cc) {
      if (cc < 7) {
#pragma unroll
        for (int r = 0; r < 4; ++r) B[(cc + 1) & 1][r] = LDS128(bbase + (6 - cc) * 512 + 64 * r);
      }
      __builtin_amdgcn_sched_barrier(0);
#pragma unroll
      for (int r = 0; r < 4; ++r)
#pragma unroll
        for (int n = cc; n < 8; ++n) acc[n] = mfma16(A[n - cc][r], B[cc & 1][r], acc[n]);
      __builtin_amdgcn_sched_barrier(0);
      LDSF4(pw + cc * 4096) = acc[cc];
      if (cc == 0 && wave < 5) {
        DRAIN();
        if (lane == 0) ST64((u32x2{tl, th}), OFF_F6 + wave * 4096 + j * 16);
      }
      if (cc == 2) f6 = LD64(f6o);
      if (cc == 4) {
        if (!TAGOK(f6)) {
          _Pragma("unroll 1") for (u32 spins = 0; spins < SPIN_CAP; ++spins) {
            CFENCE();
            f6 = LD64(f6o);
            if (TAGOK(f6)) break;
            __builtin_amdgcn_s_sleep(2);
          }
        }
      }
      if (cc == MOD_CC && wave < 5) {
#pragma unroll
        for (int i = 0; i < 6; ++i) mp[i] = LD32(OFF_MOD + i * 1280 + tid * 4);
      }
      if (cc == 7) asm volatile("" : "+v"(mp[0]), "+v"(mp[1]), "+v"(mp[2]), "+v"(mp[3]), "+v"(mp[4]), "+v"(mp[5]));
      if (cc == 4 || cc == 7) {
        BAR();
        if (cc == 4 && tid == 448) ST64((u32x2{tl, th}), OFF_P + grp * 128);
        const u32 p0 = pr + (cc == 7 ? 16384 : 0);
        const f32x4 lo = LDSF4(p0) + LDSF4(p0 + 32768), hi = LDSF4(p0 + 2048) + LDSF4(p0 + 32768 + 2048);
        ST128((u32x4{san(pkbf(lo.x, lo.y)), san(pkbf(lo.z, lo.w)), san(pkbf(hi.x, hi.y)), san(pkbf(hi.z, hi.w))}),
              yo + (cc == 7 ? 64 * 20480 : 0));
      }
    }
    STAMP(8);
  } else {
    if (wave < 5) {
      CLEARS();
      LOADQ();
      const u32 fo = OFF_F5 + (tid & 255) * 16;
      _Pragma("unroll 1") for (u32 spins = 0; spins < SPIN_CAP; ++spins) {
        CFENCE();
        const u32x2 f = LD64(fo);
        if (TAGOK(f)) break;
        __builtin_amdgcn_s_sleep(4);
      }
      DRAIN();
      if (lane == 0) ST64((u32x2{tl, th}), OFF_F6 + wave * 4096 + j * 16);
      STAMP(6);
    } else if (is_md) {
      __builtin_amdgcn_s_sleep(DLY_W);
      const int wv = wave - 5;
      const int G = wv * 8 + (lane >> 3);
      const __amdgpu_buffer_rsrc_t rm = mkrs(wmod, 640 * 3840 * 2), rc = mkrs(cvec, 1280);
      u32 cv[4];
#pragma unroll
      for (int u = 0; u < 4; ++u) cv[u] = __builtin_amdgcn_raw_buffer_load_b16(rc, (G * 27 + (lane & 7) + 8 * u) * 2, 0, 0);
      u32x4 wm[27];
      const u32 mo = G * 27 * 7680 + md_id * 128 + (lane & 7) * 16;
#pragma unroll
      for (int i = 0; i < 27; ++i) wm[i] = __builtin_amdgcn_raw_buffer_load_b128(rm, (i >= 19 && G == 23) ? 0x7000000u : mo, i * 7680, 0);
#pragma unroll
      for (int u = 0; u < 4; ++u) {
        const float x = lo16(cv[u]);
        *(LAS float*)(smem + SIL + (G * 32 + (lane & 7) + 8 * u) * 4) = rbf(x * sigm(x));
      }
      f32x2 a[4];
#pragma unroll
      for (int e = 0; e < 4; ++e) a[e] = f32x2{0.f, 0.f};
#pragma unroll
      for (int i4 = 0; i4 < 7; ++i4) {
        const f32x4 s4 = LDSF4(SIL + (G * 32 + 4 * i4) * 4);
#pragma unroll
        for (int u = 0; u < 4; ++u)
          if (4 * i4 + u < 27) {
#pragma unroll
            for (int e = 0; e < 4; ++e) a[e] += f32x2{lo16(wm[4 * i4 + u][e]), hi16(wm[4 * i4 + u][e])} * s4[u];
          }
      }
      f32x2 y2;
      y2.x = swap16_add(swap32_add(a[0].x, a[2].x), swap32_add(a[1].x, a[3].x));
      y2.y = swap16_add(swap32_add(a[0].y, a[2].y), swap32_add(a[1].y, a[3].y));
      y2.x += dppf<0x128>(y2.x); y2.y += dppf<0x128>(y2.y);
      if ((lane & 8) == 0) *(LAS f32x2*)(smem + RR + wv * 256 + (lane & 7) * 32 + lg * 8) = y2;
      DRAIN();
      STAMP(6);
    } else if (is_gu) {
      __builtin_amdgcn_s_sleep(DLY_W);
      DMA_GU();
    }
    STAMP(7);
  }

  f32x2 qf[8], cb1, sb, gb;
  if (wave < 5) {
    float V[8][2];
#pragma unroll
    for (int t = 0; t < 8; ++t) {
      qf[t] = f32x2{lo16(qd[t]), hi16(qd[t])};
      V[t][0] = qf[t].x + qf[t].y;
      V[t][1] = qf[t].x * qf[t].x + qf[t].y * qf[t].y;
    }
    reduce_tok<2>(V, 0.f, smem, RED1 + wave * 128, lane);
  }
  BAR();
  if (is_md && wave >= 5) {
    if (wave == 5) {
      const f32x2 s = *(LAS f32x2*)(smem + RR + (lane & 31) * 8) + *(LAS f32x2*)(smem + RR + 256 + (lane & 31) * 8) +
                      *(LAS f32x2*)(smem + RR + 512 + (lane & 31) * 8);
      if (lane < 32) ST32(san(pkbf(s.x, s.y)), OFF_MOD + md_id * 128 + lane * 4);
    }
    if (is_gu) { DMA_GU(); }
    else if (is_dn) {
      u32 vo = ((lane >> 1) + 32 * (wave - 5)) * 1280 + dn_id * 32 + (lane & 1) * 16;
      for (int i = wave - 5; i < 54; i += 3, vo += 96 * 1280) dma16(wd, vo, (u32)(uintptr_t)smem + i * 1024);
    }
  }
  STAMP(9);
  f32x2 x1[8];
  if (wave < 5) {
    f32x2 s = *(LAS f32x2*)(smem + RED1 + (lane & 7) * 16);
#pragma unroll
    for (int w = 1; w < 5; ++w) s += *(LAS f32x2*)(smem + RED1 + w * 128 + (lane & 7) * 16);
    const float m = s.x * (1.f / D_);
    const float rstd = __builtin_amdgcn_rsqf(fmaxf(s.y * (1.f / D_) - m * m, 0.f) + 1e-5f);
#define MODOK() (__all(umax2(umax2(umax2(mp[0], mp[1]), umax2(mp[2], mp[3])), umax2(mp[4], mp[5])) != SENT))
    if (!MODOK()) {
      _Pragma("unroll 1") for (u32 spins = 0; spins < SPIN_CAP; ++spins) {
        CFENCE();
#pragma unroll
        for (int i = 0; i < 6; ++i) mp[i] = LD32(OFF_MOD + i * 1280 + tid * 4);
        if (MODOK()) break;
        __builtin_amdgcn_s_sleep(8);
      }
    }
    CFENCE();
    const f32x2 sa = {lo16(mp[0]), hi16(mp[0])}, ga = {lo16(mp[2]), hi16(mp[2])};
    f32x2 ca1;
    { const u32 t = pkbf(1.f + lo16(mp[1]), 1.f + hi16(mp[1])); ca1 = f32x2{lo16(t), hi16(t)}; }
    sb = f32x2{lo16(mp[3]), hi16(mp[3])};
    { const u32 t = pkbf(1.f + lo16(mp[4]), 1.f + hi16(mp[4])); cb1 = f32x2{lo16(t), hi16(t)}; }
    gb = f32x2{lo16(mp[5]), hi16(mp[5])};
    const f32x2 wrf = {lo16(wrp), hi16(wrp)}, gw = cb1 * wrf, Df = {lo16(Dp), hi16(Dp)};
    f32x2 hg[8], kd[8];
#pragma unroll
    for (int t = 0; t < 8; ++t) {
      const float mt = rdl(m, t), rt = rdl(rstd, t);
      hg[t] = ((qf[t] - mt) * rt * ca1 + sa) * ga;
      const u32 u0 = kvp[0][(7 - t) >> 1], u1 = kvp[1][(7 - t) >> 1];
      kd[t] = ((t & 1) ? f32x2{lo16(u0), lo16(u1)} : f32x2{hi16(u0), hi16(u1)}) * Df * hg[t] + qf[t];
    }
    float ex = swap32_add(gw.x + gw.y, sb.x * wrf.x + sb.y * wrf.y);
    ex = swap16_add(ex, ex);
    asm volatile("" : "+v"(ex), "+v"(hg[0]), "+v"(hg[1]), "+v"(hg[2]), "+v"(hg[3]), "+v"(hg[4]), "+v"(hg[5]), "+v"(hg[6]), "+v"(hg[7]),
                 "+v"(kd[0]), "+v"(kd[1]), "+v"(kd[2]), "+v"(kd[3]), "+v"(kd[4]), "+v"(kd[5]), "+v"(kd[6]), "+v"(kd[7]));
    STAMP(10);
    if (!is_conv) {
      const u32 po = OFF_P + (j * 5 / 3) * 128;
      _Pragma("unroll 1") for (u32 spins = 0; spins < SPIN_CAP; ++spins) {
        CFENCE();
        const u32x2 f = LD64(po);
        if (TAGOK(f)) break;
        __builtin_amdgcn_s_sleep(4);
      }
      __builtin_amdgcn_s_sleep(DLY_Y);
    }
    u32x4 y0, y1;
    _Pragma("unroll 1") for (u32 spins = 0; spins < SPIN_CAP; ++spins) {
      CFENCE();
      y0 = LD128(yin); y1 = LD128(yin + 128);
      if (__all(umax2(umax4(y0), umax4(y1)) != SENT)) break;
      __builtin_amdgcn_s_sleep(2);
    }
    DRAIN();
    STAMP(11);
    float V[8][3];
#pragma unroll
    for (int t = 0; t < 8; ++t) {
      const u32 u0 = y0[t >> 1], u1 = y1[t >> 1];
      x1[t] = hg[t] * ((t & 1) ? f32x2{hi16(u0), hi16(u1)} : f32x2{lo16(u0), lo16(u1)}) + kd[t];
      V[t][0] = x1[t].x + x1[t].y;
      V[t][1] = x1[t].x * x1[t].x + x1[t].y * x1[t].y;
      V[t][2] = x1[t].x * gw.x + x1[t].y * gw.y;
    }
    reduce_tok<3>(V, ex, smem, RED2 + wave * 128, lane);
  } else {
    DRAIN();
    __builtin_amdgcn_s_setprio(3);
  }
  BAR();
  STAMP(12);
  const __amdgpu_buffer_rsrc_t ro = mkrs(out, S_ * D_ * 2);
  u32 w0 = 0, w1 = 0;
  u32 ov = 0, gv = 0;
  if (wave < 6) {
    const f32x4 s = gather5(smem, RED2, lane);
    const float Gs = rdl(s.w, 0), c0s = rdl(s.w, 4);
    const float m = s.x * (1.f / D_);
    const float rstd = __builtin_amdgcn_rsqf(fmaxf(s.y * (1.f / D_) - m * m, 0.f) + 1e-5f);
    const float r = rstd * (s.z - m * Gs) + c0s;
    u32 ob = __float_as_uint(r);
    ob ^= (u32)((int)ob >> 31) | 0x80000000u;
    ob = ob < 0xFFFFE000u ? ob : 0xFFFFE000u;
    const u32 key = (ob & 0xFFFFF000u) | ((2047u - (8 * j + (lane & 7))) << 1);
    u32 mx = umax2(key, dppu<0x124>(key)); mx = umax2(mx, dppu<0x122>(mx)); mx = umax2(mx, dppu<0x121>(mx));
    u32 k2 = key == mx ? 0u : key;
    k2 = umax2(k2, dppu<0x124>(k2)); k2 = umax2(k2, dppu<0x122>(k2)); k2 = umax2(k2, dppu<0x121>(k2));
    const u32 kA = RFL(mx), kB = RFL(k2) | 1u;
    if (wave == 5) {
      if (lane == 0) ST64((u32x2{kA, kB}), OFF_SCORE + j * 16);
      if (is_conv && lane == 0) ST64((u32x2{tl, th}), OFF_P + grp * 128 + 64);
      STAMP(1);
      if (is_gu || is_dn) {
        _Pragma("unroll 1") for (u32 spins = 0; spins < SPIN_CAP; ++spins) {
          CFENCE();
          const u32x2 f = LD64(OFF_P + (j * 5 / 3) * 128 + 64);
          if (TAGOK(f)) break;
          __builtin_amdgcn_s_sleep(2);
        }
        __builtin_amdgcn_s_sleep(DLY_SC);
        u32x2 g[4];
        _Pragma("unroll 1") for (u32 spins = 0; spins < SPIN_CAP; ++spins) {
          CFENCE();
#pragma unroll
          for (int i = 0; i < 4; ++i) g[i] = LD64(OFF_SCORE + lane * 16 + i * 1024);
          if (__all(umax2(umax2(umax2(g[0].x, g[0].y), umax2(g[1].x, g[1].y)), umax2(umax2(g[2].x, g[2].y), umax2(g[3].x, g[3].y))) != SENT)) break;
          __builtin_amdgcn_s_sleep(2);
        }
        CFENCE();
        STAMP(13);
        w0 = RFL(wave_umax(umax2(umax2(g[0].x, g[1].x), umax2(g[2].x, g[3].x))));
        u32 c2 = 0;
#pragma unroll
        for (int i = 0; i < 4; ++i) c2 = umax2(c2, g[i].x == w0 ? g[i].y : g[i].x);
        w1 = RFL(wave_umax(c2));
        const u32 wk = lane < 32 ? w0 : w1;
        const u32 tok = 2047 - ((wk >> 1) & 2047);
        if (is_gu) {
          const u32 l5 = lane & 31, o2 = 1024 + (l5 & 15) * 16;
          const u32 row = OFF_CAND + (tok >> 3) * 2560 + (wk & 1) * 1280;
          u32x4 h0, h1, h2;
          _Pragma("unroll 1") for (u32 spins = 0; spins < SPIN_CAP; ++spins) {
            CFENCE();
            h0 = LD128(row + l5 * 16); h1 = LD128(row + 512 + l5 * 16); h2 = LD128(row + o2);
            if (__all(umax2(umax2(umax4(h0), umax4(h1)), umax4(h2)) != SENT)) break;
            __builtin_amdgcn_s_sleep(1);
          }
          CFENCE();
          const u32 hb = HR + (lane >> 5) * HR_STRIDE;
          LDS128(hb + l5 * 16) = h0; LDS128(hb + 512 + l5 * 16) = h1; LDS128(hb + o2) = h2;
        } else {
          if (dn_id == 0 && lane == 0) ST64((u32x2{tl, th}), OFF_E);
          _Pragma("unroll 1") for (u32 spins = 0; spins < SPIN_CAP; ++spins) {
            CFENCE();
            const u32x2 f = LD64(OFF_RD + (tok >> 3) * 64 + (dn_id >> 3) * 8);
            if (TAGOK(f)) break;
            __builtin_amdgcn_s_sleep(1);
          }
          CFENCE();
          ov = __builtin_amdgcn_raw_buffer_load_b16(ro, tok * 1280 + dn_id * 32 + li * 2, 0, 16);
          gv = __builtin_amdgcn_raw_buffer_load_b16(rs, OFF_MOD + 5 * 1280 + dn_id * 32 + li * 2, 0, 16);
        }
      }
    } else {
      const int tA = (2047 - ((kA >> 1) & 2047)) & 7, tB = (2047 - ((kB >> 1) & 2047)) & 7;
      u32 d0 = 0, d1 = 0, op[8];
#pragma unroll
      for (int t = 0; t < 8; ++t) {
        const float mt = rdl(m, t), rt = rdl(rstd, t);
        const f32x2 h2 = (x1[t] - mt) * rt * cb1 + sb;
        const f32x2 o = gb * h2 + x1[t];
        op[t] = pkbf(o.x, o.y);
        const u32 hp = pkbf(h2.x, h2.y);
        d0 = t == tA ? hp : d0; d1 = t == tB ? hp : d1;
      }
      ST32(san(d0), OFF_CAND + j * 2560 + tid * 4);
      ST32(san(d1), OFF_CAND + j * 2560 + 1280 + tid * 4);
#pragma unroll
      for (int t = 0; t < 8; ++t) {
        if (OUT_AUX == 16 || t == tA || t == tB) __builtin_amdgcn_raw_buffer_store_b32(op[t], ro, tid * 4 + (t % 3) * 1280, j * 10240 + (t / 3) * 3840, 16);
        else __builtin_amdgcn_raw_buffer_store_b32(op[t], ro, tid * 4 + (t % 3) * 1280, j * 10240 + (t / 3) * 3840, OUT_AUX);
      }
      if (!is_gu && !is_dn) {
        DRAIN();
        if (lane == 0) ST64((u32x2{tl, th}), OFF_RD + j * 64 + wave * 8);
      }
    }
  }
  if (is_gu) {
    u32x4 Wf[20];
    if (wave < 4) {
      LAS char* p = smem + ((li >> 1) & 1) * UPB + (8 * lg + (li >> 2)) * 64 + wave * 16 + (li & 1) * 8;
#pragma unroll
      for (int s = 0; s < 20; ++s) Wf[s] = tr_frag(p + s * 2048, 256);
    }
    if (wave < 5) {
      DRAIN();
      if (lane == 0) ST64((u32x2{tl, th}), OFF_RD + j * 64 + wave * 8);
    }
    BAR();
    STAMP(14);
    if (wave < 4) {
      f32x4 a0 = {0.f, 0.f, 0.f, 0.f}, a1 = a0;
      const u32 hb = HR + (li & 1) * HR_STRIDE + lg * 16;
#pragma unroll
      for (int s = 0; s < 20; s += 2) {
        a0 = mfma16(Wf[s], LDS128(hb + s * 64), a0);
        a1 = mfma16(Wf[s + 1], LDS128(hb + s * 64 + 64), a1);
      }
      a0 += a1;
      float av[4];
#pragma unroll
      for (int e = 0; e < 4; ++e) {
        auto r = __builtin_amdgcn_permlane32_swap(__float_as_uint(a0[e]), __float_as_uint(a0[e]), false, false);
        const float g = rbf(__uint_as_float(r[0])), u = rbf(__uint_as_float(r[1]));
        av[e] = rbf(g * sigm(g)) * u;
      }
      if (li < 2 && lg < 2) ST64((u32x2{san(pkbf(av[0], av[1])), san(pkbf(av[2], av[3]))}), OFF_A + gu_id * 128 + li * 64 + wave * 16 + lg * 8);
    }
    STAMP(15);
  } else if (is_dn) {
    u32x4 Wf[7];
    LAS char* p = smem + (32 * wave + 8 * lg + (li >> 2)) * 32 + (li & 3) * 8;
#pragma unroll
    for (int s = 0; s < 6; ++s) Wf[s] = tr_frag(p + s * 8192, 128);
    Wf[6] = tr_frag(p + (wave < 6 ? 6 * 8192 : 0), 128);
    if (wave < 5) {
      DRAIN();
      if (lane == 0) ST64((u32x2{tl, th}), OFF_RD + j * 64 + wave * 8);
    }
    BAR();
    __builtin_amdgcn_s_sleep(DLY_A);
    u32x4 Bf[7];
    const u32 ao = OFF_A + wave * 128 + (li & 1) * 64 + lg * 16;
    _Pragma("unroll 1") for (u32 spins = 0; spins < SPIN_CAP; ++spins) {
      CFENCE();
      u32 mx = 0;
#pragma unroll
      for (int s = 0; s < 6; ++s) Bf[s] = LD128(ao + s * 1024);
      Bf[6] = LD128(ao + (wave < 6 ? 6 * 1024 : 0));
#pragma unroll
      for (int s = 0; s < 7; ++s) mx = umax2(mx, umax4(Bf[s]));
      if (__all(mx != SENT)) break;
      __builtin_amdgcn_s_sleep(1);
    }
    CFENCE();
    STAMP(14);
    f32x4 a0 = {0.f, 0.f, 0.f, 0.f}, a1 = a0;
#pragma unroll
    for (int s = 0; s < 6; s += 2) { a0 = mfma16(Wf[s], Bf[s], a0); a1 = mfma16(Wf[s + 1], Bf[s + 1], a1); }
    if (wave < 6) a0 = mfma16(Wf[6], Bf[6], a0);
    if (li < 2) LDSF4(RR + ((wave * 2 + li) * 16 + 4 * lg) * 4) = a0 + a1;
    BAR();
    if (wave == 5) {
      float s = 0.f;
#pragma unroll
      for (int w = 0; w < 8; ++w) s += *(LAS float*)(smem + RR + w * 128 + (lane >> 5) * 64 + li * 4);
      const u32 wk = lane < 32 ? w0 : w1, kb = wk & 0xFFFFF000u;
      const float rv = rbf(__uint_as_float((kb & 0x80000000u) ? (kb ^ 0x80000000u) : ~kb));
      const float y = rbf(rbf(s) * rbf(sigm(rv)));
      const float o = lo16(ov) + lo16(gv) * y;
      if ((lane & 16) == 0) __builtin_amdgcn_raw_buffer_store_b16((u16)pkbf(o, o), ro, (2047 - ((wk >> 1) & 2047)) * 1280 + dn_id * 32 + li * 2, 0, 16);
    }
    STAMP(15);
  }
}

extern "C" void kernel_launch(void* const* d_in, const int* in_sizes, int n_in, void* d_out, int out_size,
                              void* d_ws, size_t ws_size, hipStream_t stream) {
  (void)in_sizes; (void)n_in; (void)out_size; (void)ws_size;
  (void)hipFuncSetAttribute((const void*)fused_block, hipFuncAttributeMaxDynamicSharedMemorySize, (int)LDS_BYTES);
  fused_block<<<256, 512, LDS_BYTES, stream>>>(
      (const u16*)d_in[0], (const u16*)d_in[1], (const u16*)d_in[2], (const u16*)d_in[3], (const u16*)d_in[4],
      (const u16*)d_in[5], (const u16*)d_in[7], (const u16*)d_in[8], (const u16*)d_in[9], (const u16*)d_in[10],
      (const u16*)d_in[11], (const u16*)d_in[12], (u16*)d_out, (char*)d_ws);
}
```

```cpp
#include <hip/hip_runtime.h>
#include <hip/hip_bf16.h>
#include <stdint.h>

typedef unsigned short u16;
typedef unsigned int u32;
typedef unsigned long long u64;
typedef u32 u32x2 __attribute__((ext_vector_type(2)));
typedef u32 u32x4 __attribute__((ext_vector_type(4)));
typedef float f32x2 __attribute__((ext_vector_type(2)));
typedef float f32x4 __attribute__((ext_vector_type(4)));
typedef short s16x4 __attribute__((ext_vector_type(4)));
typedef __bf16 bf16x2 __attribute__((ext_vector_type(2)));
typedef __bf16 bf16x8 __attribute__((ext_vector_type(8)));
#define LAS __attribute__((address_space(3)))
#define GAS __attribute__((address_space(1)))
#define RLX_AGENT __ATOMIC_RELAXED, __HIP_MEMORY_SCOPE_AGENT
#define DI __device__ __forceinline__

constexpr int S_ = 2048, D_ = 640, F_ = 1728;
constexpr u32 SENT = 0xFFFFFFFFu;
constexpr u32 SPIN_CAP = 1u << 16;
constexpr u32 OFF_E = 0, OFF_FLAG = 4096, OFF_F5 = OFF_FLAG + 5 * 4096, OFF_F6 = OFF_F5 + 4096, OFF_SCORE = OFF_F6 + 5 * 4096,
              OFF_MOD = OFF_SCORE + 4096, OFF_A = OFF_MOD + 8192, OFF_RD = OFF_A + 8192, OFF_P = OFF_RD + 16384;
constexpr u32 OFF_XT = 131072, OFF_YB = OFF_XT + 2621440, OFF_CAND = OFF_YB + 2621440, OFF_END = OFF_CAND + 655360;
constexpr u32 OFF_DBG = 8u << 20;
constexpr u32 KC_STRIDE = 4160, XL = 66560, XL_STRIDE = 4640, UPB = 40960 + 32, RED1 = 86016, RED2 = RED1 + 640, RR = RED2 + 640,
              TAGW = RR + 1024, WINW = TAGW + 16, SIL = WINW + 16, HR = SIL + 3072, HR_STRIDE = 1344, KVL = HR + 2 * HR_STRIDE, PFD = KVL + 10240, P0L = PFD + 256, LDS_BYTES = P0L + 32768;
#define DLY_Y 6
#define DLY_SC 10
#define DLY_A 26
#define DLY_W 40
#define DLY_C 0
#define DLY_X 10
#define DLY_M 40
#define B0R 4
#define PF 1
#define PRI0 0
#define EARLY_Y 0
#define MODP 0
#define CPF 1
#define DLY_K 2
#define DLY_PM 0
#define DLY_CL 0
#define EPOLL 0
#define DLY_SD 8
#define WBL 0
#define KDPP 1
#define OUT_AUX 0

#define STAMP(i) do {} while (0)

DI float lo16(u32 x) { return __uint_as_float(x << 16); }
DI float hi16(u32 x) { return __uint_as_float(x & 0xffff0000u); }
DI u32 pkbf(float lo, float hi) { f32x2 f = {lo, hi}; return __builtin_bit_cast(u32, __builtin_convertvector(f, bf16x2)); }
DI float rbf(float x) { return lo16(pkbf(x, x)); }
DI u32 san(u32 x) { return x < 0xFFFFFFFEu ? x : 0xFFFFFFFEu; }
DI u32 umax2(u32 a, u32 b) { return a > b ? a : b; }
DI u32 umax4(u32x4 v) { return umax2(umax2(v.x, v.y), umax2(v.z, v.w)); }
DI float sigm(float x) { return __builtin_amdgcn_rcpf(1.f + __expf(-x)); }
template <int CTRL> DI u32 dppu(u32 x) { return (u32)__builtin_amdgcn_update_dpp(0, (int)x, CTRL, 0xf, 0xf, false); }
template <int CTRL> DI float dppf(float x) { return __uint_as_float(dppu<CTRL>(__float_as_uint(x))); }
DI float swap32_add(float a, float b) { auto r = __builtin_amdgcn_permlane32_swap(__float_as_uint(a), __float_as_uint(b), false, false); return __uint_as_float(r[0]) + __uint_as_float(r[1]); }
DI float swap16_add(float a, float b) { auto r = __builtin_amdgcn_permlane16_swap(__float_as_uint(a), __float_as_uint(b), false, false); return __uint_as_float(r[0]) + __uint_as_float(r[1]); }
DI float row_sum(float x) { x += dppf<0x128>(x); x += dppf<0x124>(x); x += dppf<0x122>(x); x += dppf<0x121>(x); return x; }
DI u32 wave_umax(u32 x) {
  x = umax2(x, dppu<0x128>(x)); x = umax2(x, dppu<0x124>(x)); x = umax2(x, dppu<0x122>(x)); x = umax2(x, dppu<0x121>(x));
  auto r = __builtin_amdgcn_permlane16_swap(x, x, false, false); x = umax2(r[0], r[1]);
  auto s = __builtin_amdgcn_permlane32_swap(x, x, false, false); return umax2(s[0], s[1]);
}
DI f32x4 mfma16(u32x4 a, u32x4 b, f32x4 c) { return __builtin_amdgcn_mfma_f32_16x16x32_bf16(__builtin_bit_cast(bf16x8, a), __builtin_bit_cast(bf16x8, b), c, 0, 0, 0); }
#define LDS128(off) (*(LAS u32x4*)(smem + (off)))
#define LDSF4(off) (*(LAS f32x4*)(smem + (off)))
#define LDS32(off) (*(LAS u32*)(smem + (off)))
#define RFL(x) ((u32)__builtin_amdgcn_readfirstlane((int)(x)))
#define CFENCE() asm volatile("" ::: "memory")

#define DPP_ALL " row_mask:0xf bank_mask:0xf\n\t"
DI void quad_tr(u32x4& p, u32x4& q) {
  u32 a0, a1, a2, a3, b0, b1, b2, b3;
  asm volatile(
      "s_mov_b32 vcc_lo, 0x55555555\n\ts_mov_b32 vcc_hi, 0x55555555\n\ts_nop 0\n\t"
      "v_cndmask_b32_dpp %0, %9, %8, vcc quad_perm:[1,0,3,2]" DPP_ALL "v_cndmask_b32_dpp %2, %11, %10, vcc quad_perm:[1,0,3,2]" DPP_ALL
      "v_cndmask_b32_dpp %4, %13, %12, vcc quad_perm:[1,0,3,2]" DPP_ALL "v_cndmask_b32_dpp %6, %15, %14, vcc quad_perm:[1,0,3,2]" DPP_ALL
      "s_not_b64 vcc, vcc\n\t"
      "v_cndmask_b32_dpp %1, %8, %9, vcc quad_perm:[1,0,3,2]" DPP_ALL "v_cndmask_b32_dpp %3, %10, %11, vcc quad_perm:[1,0,3,2]" DPP_ALL
      "v_cndmask_b32_dpp %5, %12, %13, vcc quad_perm:[1,0,3,2]" DPP_ALL "v_cndmask_b32_dpp %7, %14, %15, vcc quad_perm:[1,0,3,2]" DPP_ALL
      "s_mov_b32 vcc_lo, 0x33333333\n\ts_mov_b32 vcc_hi, 0x33333333\n\t"
      "v_cndmask_b32_dpp %8, %2, %0, vcc quad_perm:[2,3,0,1]" DPP_ALL "v_cndmask_b32_dpp %12, %6, %4, vcc quad_perm:[2,3,0,1]" DPP_ALL
      "v_cndmask_b32_dpp %9, %3, %1, vcc quad_perm:[2,3,0,1]" DPP_ALL "v_cndmask_b32_dpp %13, %7, %5, vcc quad_perm:[2,3,0,1]" DPP_ALL
      "s_not_b64 vcc, vcc\n\t"
      "v_cndmask_b32_dpp %10, %0, %2, vcc quad_perm:[2,3,0,1]" DPP_ALL "v_cndmask_b32_dpp %11, %1, %3, vcc quad_perm:[2,3,0,1]" DPP_ALL
      "v_cndmask_b32_dpp %14, %4, %6, vcc quad_perm:[2,3,0,1]" DPP_ALL "v_cndmask_b32_dpp %15, %5, %7, vcc quad_perm:[2,3,0,1]" DPP_ALL
      : "=&v"(a0), "=&v"(a1), "=&v"(a2), "=&v"(a3), "=&v"(b0), "=&v"(b1), "=&v"(b2), "=&v"(b3),
        "+v"(p.x), "+v"(p.y), "+v"(p.z), "+v"(p.w), "+v"(q.x), "+v"(q.y), "+v"(q.z), "+v"(q.w)
      :
      : "vcc");
}
DI f32x2 up2(u32 x) { return f32x2{lo16(x), hi16(x)}; }
DI float hsum(f32x2 a) { return a.x + a.y; }
struct Row5 { u32x4 a; u32 b; };
DI u32 r5(const Row5& r, int i) { return i < 4 ? r.a[i] : r.b; }
DI u32 umax5(const Row5& r) { return umax2(umax4(r.a), r.b); }
template <int AUX> DI Row5 ld5(__amdgpu_buffer_rsrc_t rs, u32 vo, u32 so) {
  return Row5{__builtin_amdgcn_raw_buffer_load_b128(rs, vo, so, AUX), __builtin_amdgcn_raw_buffer_load_b32(rs, vo + 16, so, AUX)};
}
template <int AUX> DI void st5(const Row5& r, __amdgpu_buffer_rsrc_t rs, u32 vo, u32 so) {
  __builtin_amdgcn_raw_buffer_store_b128(r.a, rs, vo, so, AUX);
  __builtin_amdgcn_raw_buffer_store_b32(r.b, rs, vo + 16, so, AUX);
}
DI void ln1_partial(const Row5 (&qd)[2], LAS char* smem, int wave, int lane) {
  float a[2][2];
#pragma unroll
  for (int s = 0; s < 2; ++s) {
    const f32x2 x0 = up2(qd[s].a.x), x1 = up2(qd[s].a.y), x2 = up2(qd[s].a.z), x3 = up2(qd[s].a.w), x4 = up2(qd[s].b);
    a[s][0] = hsum(((x0 + x1) + (x2 + x3)) + x4);
    a[s][1] = hsum(x0 * x0 + (x1 * x1 + (x2 * x2 + (x3 * x3 + x4 * x4))));
  }
  float R = swap16_add(swap32_add(a[0][0], a[1][0]), swap32_add(a[0][1], a[1][1]));
  asm volatile("s_nop 1\n\tv_add_f32_dpp %0, %0, %0 row_ror:4" DPP_ALL "s_nop 1\n\tv_add_f32_dpp %0, %0, %0 row_ror:8" DPP_ALL : "+v"(R));
  if ((lane & 12) == 0) LDS32(RED1 + wave * 64 + (2 * (lane & 3) + (lane >> 5)) * 8 + ((lane >> 4) & 1) * 4) = __float_as_uint(R);
}
DI float rdl(float x, int l) { return __uint_as_float((u32)__builtin_amdgcn_readlane((int)__float_as_uint(x), l)); }

template <int M>
DI void load_afrags(u32 addr, u32x4 (&A)[8][4]) {
  u32x2 o[16];
  asm volatile(
      "ds_read_b64 %0, %16 offset:%17\n\tds_read_b64 %1, %16 offset:%17+8\n\t"
      "ds_read_b64 %2, %16 offset:%17+64\n\tds_read_b64 %3, %16 offset:%17+72\n\t"
      "ds_read_b64 %4, %16 offset:%17+128\n\tds_read_b64 %5, %16 offset:%17+136\n\t"
      "ds_read_b64 %6, %16 offset:%17+192\n\tds_read_b64 %7, %16 offset:%17+200\n\t"
      "ds_read_b64 %8, %16 offset:%17+512\n\tds_read_b64 %9, %16 offset:%17+520\n\t"
      "ds_read_b64 %10, %16 offset:%17+576\n\tds_read_b64 %11, %16 offset:%17+584\n\t"
      "ds_read_b64 %12, %16 offset:%17+640\n\tds_read_b64 %13, %16 offset:%17+648\n\t"
      "ds_read_b64 %14, %16 offset:%17+704\n\tds_read_b64 %15, %16 offset:%17+712\n\t"
      "s_waitcnt lgkmcnt(0)"
      : "=&v"(o[0]), "=&v"(o[1]), "=&v"(o[2]), "=&v"(o[3]), "=&v"(o[4]), "=&v"(o[5]), "=&v"(o[6]), "=&v"(o[7]),
        "=&v"(o[8]), "=&v"(o[9]), "=&v"(o[10]), "=&v"(o[11]), "=&v"(o[12]), "=&v"(o[13]), "=&v"(o[14]), "=&v"(o[15])
      : "v"(addr), "i"(M * 512)
      : "memory");
#pragma unroll
  for (int r = 0; r < 4; ++r) {
    A[M][r] = u32x4{o[2 * r].x, o[2 * r].y, o[2 * r + 1].x, o[2 * r + 1].y};
    A[M + 1][r] = u32x4{o[8 + 2 * r].x, o[8 + 2 * r].y, o[8 + 2 * r + 1].x, o[8 + 2 * r + 1].y};
  }
}
DI u32x4 tr_frag(LAS char* p, u32 step) {
  typedef short v4i16_t __attribute__((ext_vector_type(4)));
  u32x2 a = __builtin_bit_cast(u32x2, __builtin_amdgcn_ds_read_tr16_b64_v4i16((LAS v4i16_t*)p));
  u32x2 b = __builtin_bit_cast(u32x2, __builtin_amdgcn_ds_read_tr16_b64_v4i16((LAS v4i16_t*)(p + step)));
  return u32x4{a.x, a.y, b.x, b.y};
}

DI void dma16(const void* base, u32 voff, u32 lds) {
  u32 keep;
  asm volatile("s_mov_b32 %0, m0\n\ts_mov_b32 m0, %3\n\ts_nop 0\n\tglobal_load_lds_dwordx4 %1, %2\n\ts_mov_b32 m0, %0"
               : "=&s"(keep) : "v"(voff), "s"(base), "s"(lds) : "memory");
}
DI void dma4(const void* base, u32 voff, u32 lds) {
  u32 keep;
  asm volatile("s_mov_b32 %0, m0\n\ts_mov_b32 m0, %3\n\ts_nop 0\n\tglobal_load_lds_dword %1, %2\n\ts_mov_b32 m0, %0"
               : "=&s"(keep) : "v"(voff), "s"(base), "s"(lds) : "memory");
}
#define BAR() asm volatile("s_waitcnt lgkmcnt(0)\n\ts_barrier" ::: "memory")
#define DRAIN() do { CFENCE(); __builtin_amdgcn_s_waitcnt(0x0F70); CFENCE(); } while (0)
DI __amdgpu_buffer_rsrc_t mkrs(const void* p, u32 bytes) { return __builtin_amdgcn_make_buffer_rsrc((void*)p, (short)0, (int)bytes, 0x00020000); }

template <int CC>
DI void conv_round(const u32x4 (&A)[8][4], u32x4 (&B)[3][4], f32x4 (&acc)[8], LAS char* smem, u32 bbase, u32 pw) {
  constexpr int cur = CC == 7 ? 2 : (CC & 1);
#pragma unroll
  for (int r = 0; r < 4; ++r) {
#pragma unroll
    for (int n = CC; n < 8; ++n) acc[n] = mfma16(A[n - CC][r], B[cur][r], acc[n]);
    __builtin_amdgcn_sched_barrier(0);
    if (r == 0) {
      if (CC > 0) LDSF4(pw + (CC - 1) * 4096) = acc[CC - 1];
      if (CC < 6) {
#pragma unroll
        for (int i = 0; i < 4; ++i) B[(CC + 1) & 1][i] = LDS128(bbase + (6 - CC) * 512 + 64 * i);
      }
      if (CC == 5) {
#pragma unroll
        for (int i = 0; i < 4; ++i) B[2][i] = LDS128(bbase + 64 * i);
      }
      __builtin_amdgcn_sched_barrier(0);
    }
  }
#pragma unroll
  for (int n = CC; n < 8; ++n) asm volatile("" : "+v"(acc[n]));
  if (CC == 7) LDSF4(pw + 7 * 4096) = acc[7];
}

__global__ void __launch_bounds__(512, 2)
fused_block(const u16* __restrict__ q, const u16* __restrict__ k, const u16* __restrict__ v,
            const u16* __restrict__ shiftk, const u16* __restrict__ ssmk, const u16* __restrict__ ssmD,
            const u16* __restrict__ wr, const u16* __restrict__ wg, const u16* __restrict__ wu,
            const u16* __restrict__ wd, const u16* __restrict__ cvec, const u16* __restrict__ wmod,
            u16* __restrict__ out, char* __restrict__ ws) {
  extern __shared__ __attribute__((aligned(16))) char smem_g[];
  LAS char* smem = (LAS char*)smem_g;
  if (CPF) asm volatile(".Lcode_at:");
  STAMP(0);
  const int tid = threadIdx.x, lane = tid & 63;
  const int wave = __builtin_amdgcn_readfirstlane(tid >> 6);
  const int b = blockIdx.x, xcd = b & 7, slot = b >> 3;
  const bool is_conv = slot < 20;
  const int grp = xcd * 20 + slot;
  const int cm = (slot - 20) >> 31;
  const int j = ((96 + grp) & cm) | ((xcd * 12 + slot - 20) & ~cm);
#define ROLES() int sa_ = slot - 20; asm volatile("" : "+s"(sa_)); \
  const int nd = xcd < 2 ? 8 : 4, gu_u = sa_ - nd; \
  const int dn_id = (xcd < 2 ? 8 * xcd : 8 + 4 * xcd) + sa_, gu_id = (xcd < 2 ? 4 * xcd : 8 * xcd - 8) + gu_u; \
  const bool is_dn = !is_conv && gu_u < 0, is_gu = gu_u >= 0 && gu_id < 54, is_md = !is_conv && (gu_u < 0 || gu_u >= (xcd < 2 ? 3 : 5)); \
  const int md_id = gu_u < 0 ? dn_id : (xcd < 2 ? 40 + xcd : 31 + 3 * xcd + gu_u)
  asm volatile("" ::"s"(q), "s"(k), "s"(v), "s"(shiftk), "s"(ssmk), "s"(ssmD), "s"(wr), "s"(wg), "s"(wu), "s"(wd),
               "s"(cvec), "s"(wmod), "s"(out), "s"(ws));
  const __amdgpu_buffer_rsrc_t rs = mkrs(ws, OFF_END);
#define LD128(off) __builtin_amdgcn_raw_buffer_load_b128(rs, (off), 0, 16)
#define LD64(off) __builtin_amdgcn_raw_buffer_load_b64(rs, (off), 0, 16)
#define LD32(off) __builtin_amdgcn_raw_buffer_load_b32(rs, (off), 0, 16)
#define ST128(val, off) __builtin_amdgcn_raw_buffer_store_b128((val), rs, (off), 0, 16)
#define ST64(val, off) __builtin_amdgcn_raw_buffer_store_b64((val), rs, (off), 0, 16)
#define ST32(val, off) __builtin_amdgcn_raw_buffer_store_b32((val), rs, (off), 0, 16)
  const u32x4 SENT4 = {SENT, SENT, SENT, SENT};
  const int li = lane & 15, lg = lane >> 4;
  const u32 ybase = OFF_YB + (j >> 1) * 20480 + (j & 1) * 64;

  u32 tl, th;
  Row5 qd[2], Dp, wrp, mp[6];
  const __amdgpu_buffer_rsrc_t rq = mkrs(q, S_ * D_ * 2);
  const u32 cho = (tid >> 2) * 20;
#define LOADQ() do { qd[0] = ld5<0>(rq, cho + (tid & 3) * 2560, j * 10240); qd[1] = ld5<0>(rq, cho + (tid & 3) * 2560 + 1280, j * 10240); } while (0)
#define LOADDW() do { Dp = ld5<0>(mkrs(ssmD, 1280), cho, 0); wrp = ld5<0>(mkrs(wr, 1280), cho, 0); } while (0)
#define LOADMOD() _Pragma("unroll") for (int i = 0; i < 6; ++i) mp[i] = ld5<16>(rs, OFF_MOD + i * 1280 + cho, 0)
#define MKTAG(e) do { u32 l_, h_; asm volatile("s_waitcnt vmcnt(0)\n\tv_readfirstlane_b32 %0, %2\n\tv_readfirstlane_b32 %1, %3" : "=s"(l_), "=s"(h_) : "v"((e).x), "v"((e).y) : "memory"); \
    DRAIN(); const u64 t_ = (((u64)h_ << 32) | l_) + 1; tl = (u32)t_; th = (u32)(t_ >> 32); } while (0)
#define CLEAR1(t) do { const u32 yc = ybase + ((t) >> 2) * 256 + ((t) & 3) * 16; ST128(SENT4, yc); ST128(SENT4, yc + 128); \
    if (((t) & 2) == 0) ST128(SENT4, OFF_CAND + j * 2560 + ((t) & 1) * 1280 + ((t) >> 2) * 16); } while (0)
#define CLEARS() do { CLEAR1(tid & 255); if ((wave & 3) == 0) CLEAR1(256 + lane); } while (0)
#define F6SET() do { DRAIN(); if (lane < ((wave & 3) ? 1 : 2)) ST64((u32x2{tl, th}), OFF_F6 + ((wave & 3) + 4 * lane) * 4096 + j * 16); } while (0)
#define DMA_GU() do { u32 vo = ((lane >> 2) + 16 * (wave - 5)) * 3456 + gu_id * 64 + (lane & 3) * 16; const u32 lb = (u32)(uintptr_t)smem; \
    for (int i = wave - 5; i < 40; i += 3, vo += 48 * 3456) { dma16(wg, vo, lb + i * 1024); dma16(wu, vo, lb + UPB + i * 1024); } } while (0)
#define TAGOK(f) (__all((((f).x ^ tl) | ((f).y ^ th)) == 0))
  u32x2 e;
  asm volatile("global_load_dwordx2 %0, %1, %2 sc1" : "=v"(e) : "v"(0u), "s"(ws) : "memory");
  u32x4 kw[8];
  if (is_conv && (u32)(wave - 5) < 2) {
    if (DLY_K) __builtin_amdgcn_s_sleep(DLY_K);
    const __amdgpu_buffer_rsrc_t rK = mkrs(ssmk, S_ * D_ * 2);
    const int e0 = (8 * (grp >> 1) + 4 * (wave - 5) + (grp & 1)) * 4096 + lane * 16;
#pragma unroll
    for (int i = 0; i < 8; ++i) kw[i] = __builtin_amdgcn_raw_buffer_load_b128(rK, e0 + (i & 3) * 1024, (i >> 2) * 8192, 0);
    CFENCE();
  }
  if (__builtin_expect(wave < 5, 1)) {
    u32 kk[11], vv[8];
    const __amdgpu_buffer_rsrc_t rk = mkrs(k, S_ * D_ * 2), rv = mkrs(v, S_ * D_ * 2);
    const int r0 = j * 10240, rh = j ? r0 - 3840 : 0;
#pragma unroll
    for (int i = 0; i < 3; ++i) kk[i] = __builtin_amdgcn_raw_buffer_load_b32(rk, tid * 4 + i * 1280, rh, 0);
#pragma unroll
    for (int i = 0; i < 8; ++i) kk[3 + i] = __builtin_amdgcn_raw_buffer_load_b32(rk, tid * 4 + (i % 3) * 1280, r0 + (i / 3) * 3840, 0);
#pragma unroll
    for (int i = 0; i < 8; ++i) vv[i] = __builtin_amdgcn_raw_buffer_load_b32(rv, tid * 4 + (i % 3) * 1280, r0 + (i / 3) * 3840, 0);
    const u32x4 shv = *(const u32x4*)(shiftk + 8 * tid);
    __builtin_amdgcn_sched_barrier(0);
    if (j == 0) { kk[0] = 0; kk[1] = 0; kk[2] = 0; }
    const f32x2 s0 = {lo16(shv.x), lo16(shv.z)}, s1 = {hi16(shv.x), hi16(shv.z)}, s2 = {lo16(shv.y), lo16(shv.w)}, s3 = {hi16(shv.y), hi16(shv.w)};
    f32x2 kf[11], kv[8];
    u32x4 kvp[2];
#pragma unroll
    for (int i = 0; i < 11; ++i) kf[i] = f32x2{lo16(kk[i]), hi16(kk[i])};
#pragma unroll
    for (int t = 0; t < 8; ++t) {
      f32x2 a = kf[t] * s3;
      a = kf[t + 1] * s2 + a;
      a = kf[t + 2] * s1 + a;
      a = kf[t + 3] * s0 + a;
      kv[t] = a * f32x2{lo16(vv[t]), hi16(vv[t])};
    }
#pragma unroll
    for (int d = 0; d < 4; ++d) {
      kvp[0][d] = pkbf(kv[7 - 2 * d].x, kv[6 - 2 * d].x);
      kvp[1][d] = pkbf(kv[7 - 2 * d].y, kv[6 - 2 * d].y);
    }
    const u32 xo = OFF_XT + (tid >> 2) * 32768 + j * 128 + (tid & 3) * 16;
    ST128(kvp[0], xo);
    ST128(kvp[1], xo + 64);
    LDS128(KVL + tid * 32) = u32x4{kvp[0].w, kvp[1].w, kvp[0].z, kvp[1].z};
    LDS128(KVL + tid * 32 + 16) = u32x4{kvp[0].y, kvp[1].y, kvp[0].x, kvp[1].x};
    STAMP(2);
    MKTAG(e);
    asm volatile("" ::"v"(kw[0]), "v"(kw[1]), "v"(kw[2]), "v"(kw[3]), "v"(kw[4]), "v"(kw[5]), "v"(kw[6]), "v"(kw[7]));
    if (lane == 0) ST64((u32x2{tl, th}), OFF_FLAG + wave * 4096 + j * 16);
    STAMP(3);
  } else if (wave < 7) {
    if (is_conv) {
      u32x2 kx[8];
#pragma unroll
      for (int i = 0; i < 8; ++i) {
        const int c = (wave - 5) * 2 + (i >> 2), a = (i & 3) * 64 + lane;
        {
          const u32 nx = (i & 3) < 3 ? RFL(kw[(i + 1) & 7].x) : 0u, ny = (i & 3) < 3 ? RFL(kw[(i + 1) & 7].y) : 0u;
          kx[i].x = (u32)__builtin_amdgcn_update_dpp((int)nx, (int)kw[i].x, 0x130, 0xf, 0xf, false);
          kx[i].y = (u32)__builtin_amdgcn_update_dpp((int)ny, (int)kw[i].y, 0x130, 0xf, 0xf, false);
        }
        const u32 w0 = kw[i].x, w1 = kw[i].y, w2 = kw[i].z, w3 = kw[i].w, w4 = kx[i].x, w5 = kx[i].y;
        const u32 a0 = __builtin_amdgcn_alignbyte(w1, w0, 2), a1 = __builtin_amdgcn_alignbyte(w2, w1, 2),
                  a2 = __builtin_amdgcn_alignbyte(w3, w2, 2), a3 = __builtin_amdgcn_alignbyte(w4, w3, 2),
                  a4 = __builtin_amdgcn_alignbyte(w5, w4, 2);
        const u32 base = c * 4 * KC_STRIDE + (a + 2) * 16;
        if (i == 0) STAMP(2);
        LDS128(base) = kw[i];
        LDS128(base + KC_STRIDE) = u32x4{a0, a1, a2, a3};
        LDS128(base + 2 * KC_STRIDE) = u32x4{w1, w2, w3, w4};
        LDS128(base + 3 * KC_STRIDE) = u32x4{a1, a2, a3, a4};
        if ((i & 3) == 0 && lane == 0) {
          LDS128(base - 16 + KC_STRIDE) = u32x4{0, 0, 0, w0 << 16};
          LDS128(base - 16 + 2 * KC_STRIDE) = u32x4{0, 0, 0, w0};
          LDS128(base - 16 + 3 * KC_STRIDE) = u32x4{0, 0, w0 << 16, a0};
        }
      }
      STAMP(3);
      MKTAG(e);
    } else {
      if (DLY_CL) __builtin_amdgcn_s_sleep(DLY_CL);
      for (int u = wave - 5; u < 5; u += 2) CLEAR1(u * 64 + lane);
      MKTAG(e);
      if (lane < 8 - wave) ST64((u32x2{tl, th}), OFF_F6 + (wave - 5 + 2 * lane) * 4096 + j * 16);
      STAMP(3);
    }
  } else {
    if (CPF) {
      const char* pc; int back, len;
      asm volatile("s_getpc_b64 %0\n.Lcode_from:\n\ts_mov_b32 %1, .Lcode_from-.Lcode_at\n\ts_mov_b32 %2, .Lcode_to-.Lcode_at" : "=s"(pc), "=s"(back), "=s"(len));
      const int o = (slot * 8 + lane) * 128;
      if (lane < 8 && o < len) dma4(pc - back, o, (u32)(uintptr_t)smem + PFD);
    }
    if (lane == 0) ST64((u32x2{SENT, SENT}), OFF_SCORE + j * 16);
    ROLES();
    (void)is_dn;
    if (lane < 8) {
      if (is_md) ST128(SENT4, OFF_MOD + md_id * 128 + lane * 16);
      if (is_gu) ST128(SENT4, OFF_A + gu_id * 128 + lane * 16);
    }
    if (is_conv) {
      const u32x4 z = {0, 0, 0, 0};
      if (lane < 60) {
        LDS128(XL + (lane / 30) * XL_STRIDE + (256 + lane % 30) * 16) = z;
        LDS128(XL + (lane / 30 + 2) * XL_STRIDE + (256 + lane % 30) * 16) = z;
      }
      if (lane < 20) LDS128(lane < 16 ? lane * KC_STRIDE : (lane - 16) * 4 * KC_STRIDE + 16) = z;
    }
    MKTAG(e);
    if (lane == 0) ST64((u32x2{tl, th}), OFF_F5 + j * 16);
    if (DLY_PM && is_conv) {
      __builtin_amdgcn_s_sleep(DLY_PM);
      for (int u = slot; u < (xcd < 2 ? 90 : 70); u += 20) {
        const int i = u / 10, id = i < nd ? dn_id - sa_ + i : (xcd < 2 ? 40 + xcd : 32 + 3 * xcd + i);
        dma4(wmod, ((u - 10 * i) * 64 + lane) * 7680 + id * 128, (u32)(uintptr_t)smem + PFD);
      }
    }
    STAMP(3);
  }

  ROLES();
  const int tk = 2 * (lane & 3) + ((lane >> 2) & 1);
  const bool odd4 = lane & 4;
  float rs1[2], nm1[2];
  const u32 pa = 5 * (tid >> 2) + (tid & 3), pb = 5 * (tid >> 2) + 4;
  const u32 ya = ybase + (pa >> 2) * 256 + (pa & 3) * 16, yb = ybase + (pb >> 2) * 256 + (pb & 3) * 16 + (tid & 3) * 4;
  Row5 y0, y1;
#define LOADY() do { y0.a = LD128(ya); y1.a = LD128(ya + 128); y0.b = LD32(yb); y1.b = LD32(yb + 128); } while (0)
#define YOK() (__all(umax2(umax5(y0), umax5(y1)) != SENT))
#define PINMOD() asm volatile("" : "+v"(mp[0].a), "+v"(mp[0].b), "+v"(mp[1].a), "+v"(mp[1].b), "+v"(mp[2].a), "+v"(mp[2].b), "+v"(mp[3].a), "+v"(mp[3].b), \
    "+v"(mp[4].a), "+v"(mp[4].b), "+v"(mp[5].a), "+v"(mp[5].b), "+v"(Dp.a), "+v"(Dp.b), "+v"(wrp.a), "+v"(wrp.b))
  u32x2 kp[5];
#define LNSTATS() do { \
    const f32x2 s = (*(LAS f32x2*)(smem + RED1 + tk * 8) + *(LAS f32x2*)(smem + RED1 + 64 + tk * 8)) + \
                    (*(LAS f32x2*)(smem + RED1 + 128 + tk * 8) + *(LAS f32x2*)(smem + RED1 + 192 + tk * 8)); \
    _Pragma("unroll") for (int c = 0; c < 5; ++c) kp[c] = *(LAS u32x2*)(smem + KVL + (tid >> 2) * 160 + c * 32 + (tid & 3) * 8); \
    const float m0 = s.x * (1.f / D_), m = rbf(m0); \
    const float rstd = __builtin_amdgcn_rcpf(rbf(__builtin_amdgcn_sqrtf(rbf(rbf(fmaxf(s.y * (1.f / D_) - m0 * m0, 0.f)) + 1e-5f)))), nmr = -m * rstd; \
    const float r2 = dppf<0x124>(rstd), n2 = dppf<0x124>(nmr); \
    rs1[0] = odd4 ? r2 : rstd; rs1[1] = odd4 ? rstd : r2; nm1[0] = odd4 ? n2 : nmr; nm1[1] = odd4 ? nmr : n2; \
    asm volatile("" : "+v"(rs1[0]), "+v"(rs1[1]), "+v"(nm1[0]), "+v"(nm1[1])); } while (0)
  if (is_conv) {
    const int c = wave & 3, hf = wave >> 2;
    if (hf == 0) LOADQ();
    BAR();
    STAMP(4);
    const u32 aaddr = (u32)(uintptr_t)smem + (c * 4 + ((li + 1) & 3)) * KC_STRIDE + 16 * lg + 8 * ((li + 1) >> 2) + 256 * hf;
#define AFRAGS() u32x4 A[8][4]; load_afrags<0>(aaddr, A); load_afrags<2>(aaddr, A); load_afrags<4>(aaddr, A); load_afrags<6>(aaddr, A)
#define BFIRST() _Pragma("unroll") for (int r = 0; r < 4; ++r) B[0][r] = LDS128(bbase + 7 * 512 + 64 * r)
    u32 bbase = XL + c * XL_STRIDE + (30 + 16 * hf - 2 * li + lg) * 16;
    asm("" : "+v"(bbase));
    const u32 pw = (hf ? 32768 : P0L) + (lg & 1) * 2048 + (c * 32 + ((2 * li + (lg >> 1) + 4 * c) & 31)) * 16;
    f32x4 acc[8];
#pragma unroll
    for (int n = 0; n < 8; ++n) acc[n] = f32x4{0.f, 0.f, 0.f, 0.f};
    u32x4 B[3][4];
#define ROUND(n) conv_round<n>(A, B, acc, smem, bbase, pw)
#define B0(n) if (B0R == n) asm volatile("s_barrier" ::: "memory")
    const u32 t8 = tid & 255;
    const u32 pr = (t8 >> 7) * 4096 + ((t8 & 3) * 32 + ((((t8 >> 2) & 31) + 4 * (t8 & 3)) & 31)) * 16;
    const u32 yo = OFF_YB + (t8 >> 3) * 20480 + grp * 128 + (t8 & 7) * 16;
#define PASS(p) do { const u32 p0 = pr + (p) * 8192; \
      const f32x4 lo = LDSF4(p0 + P0L) + LDSF4(p0 + 32768), hi = LDSF4(p0 + P0L + 2048) + LDSF4(p0 + 32768 + 2048); \
      ST128((u32x4{san(pkbf(lo.x, lo.y)), san(pkbf(lo.z, lo.w)), san(pkbf(hi.x, hi.y)), san(pkbf(hi.z, hi.w))}), yo + (p) * 32 * 20480); } while (0)
    if (hf == 0) {
      AFRAGS();
      STAMP(5);
      DRAIN();
      ln1_partial(qd, smem, wave, lane);
      STAMP(6);
      BAR();
      STAMP(7);
      BFIRST();
      __builtin_amdgcn_s_setprio(2);
      ROUND(0); ROUND(1); B0(1); ROUND(2); B0(2); ROUND(3); B0(3); ROUND(4); B0(4); ROUND(5); B0(5);
      const u32 f6o = OFF_F6 + (grp >> 5) * 4096 + tid * 16;
      u32x2 f6 = LD64(f6o);
      LOADDW();
      __builtin_amdgcn_sched_barrier(0);
      ROUND(6); ROUND(7);
      __builtin_amdgcn_s_setprio(PRI0);
      STAMP(9);
      LNSTATS();
      if (!TAGOK(f6)) {
        _Pragma("unroll 1") for (u32 spins = 0; spins < SPIN_CAP; ++spins) {
          CFENCE();
          f6 = LD64(f6o);
          if (TAGOK(f6)) break;
          __builtin_amdgcn_s_sleep(2);
        }
      }
      CFENCE();
      STAMP(13);
      BAR();
      PASS(0);
      if (MODP == 0) { LOADMOD(); }
      BAR();
      STAMP(15);
      PASS(1);
      if (tid == 192) ST64((u32x2{tl, th}), OFF_P + grp * 128);
      if (MODP == 1) { LOADMOD(); }
      BAR();
      STAMP(1);
      PASS(2);
      if (EARLY_Y) LOADY(); else y0 = y1 = Row5{SENT4, SENT};
      PINMOD();
    } else {
      if (DLY_X) __builtin_amdgcn_s_sleep(DLY_X);
      {
        const u32 fo = OFF_FLAG + (grp >> 5) * 4096 + (tid - 256) * 16, go = OFF_F5 + (tid - 256) * 16;
        const bool own = tid - 256 == j;
        _Pragma("unroll 1") for (u32 spins = 0; spins < SPIN_CAP; ++spins) {
          CFENCE();
          const u32x2 f = LD64(fo), g = LD64(go);
          if (__all(own || ((f.x ^ tl) | (f.y ^ th) | (g.x ^ tl) | (g.y ^ th)) == 0)) break;
          __builtin_amdgcn_s_sleep(2);
        }
        DRAIN();
        STAMP(6);
        const u32 xo = OFF_XT + (grp >> 1) * 32768 + (grp & 1) * 64 + (c * 64 + (lane >> 2)) * 128 + (lane & 3) * 16;
        const u32 xa = XL + (lane & 3) * XL_STRIDE + (255 - 48 - 64 * c - (lane >> 2)) * 16;
        u32x4 x[4];
#pragma unroll
        for (int i = 0; i < 4; ++i) x[i] = LD128(xo + i * 2048);
#pragma unroll
        for (int i = 0; i < 4; ++i) LDS128(xa + (3 - i) * 256) = x[i];
        BAR();
        STAMP(7);
      }
      CLEARS();
      AFRAGS();
      BFIRST();
      F6SET();
      if (PF && wave > 4) {
        const int u = slot * 3 + wave - 5, n = u * 64 + lane;
        const u32 pfd = (u32)(uintptr_t)smem + PFD;
        if (xcd < 2) { if (u < 54) dma4(wd, (n >> 1) * 1280 + (2 * xcd + (n & 1)) * 128, pfd); }
        else if (u < 27) dma4(wd, n * 1280 + (2 + xcd) * 128, pfd);
        else if (u < 47 && xcd < 7) dma4(u < 37 ? wg : wu, (n - (u < 37 ? 1728 : 2368)) * 3456 + (8 * xcd - 2) * 64, pfd);
      }
      __builtin_amdgcn_s_setprio(1);
      if (B0R) asm volatile("s_barrier" ::: "memory");
      STAMP(9);
      ROUND(0); ROUND(1); ROUND(2);
      asm volatile("s_waitcnt lgkmcnt(4)\n\ts_barrier" ::: "memory");
      ROUND(3); ROUND(4);
      STAMP(10);
      asm volatile("s_waitcnt lgkmcnt(4)\n\ts_barrier" ::: "memory");
      STAMP(11);
      ROUND(5); ROUND(6); ROUND(7);
      BAR();
      PASS(3);
    }
    __builtin_amdgcn_s_setprio(0);
    STAMP(8);
  } else {
    if (wave < 4) { LOADQ(); LOADDW(); }
    BAR();
    if (wave < 4) {
      const u32 fo = OFF_F5 + tid * 16;
      _Pragma("unroll 1") for (u32 spins = 0; spins < SPIN_CAP; ++spins) {
        CFENCE();
        const u32x2 f = LD64(fo);
        if (TAGOK(f)) break;
        __builtin_amdgcn_s_sleep(4);
      }
      DRAIN();
      ln1_partial(qd, smem, wave, lane);
      STAMP(6);
    } else if (is_md) {
      const int wv = wave - 4;
      const int G = wv * 8 + (lane >> 3);
      const __amdgpu_buffer_rsrc_t rm = mkrs(wmod, 640 * 3840 * 2), rc = mkrs(cvec, 1280);
      u32 cv[3];
#pragma unroll
      for (int u = 0; u < 3; ++u) cv[u] = __builtin_amdgcn_raw_buffer_load_b16(rc, (G * 20 + (lane & 7) + 8 * u) * 2, 0, 0);
      u32x4 wm[20];
      const u32 mo = G * 20 * 7680 + md_id * 128 + (lane & 7) * 16;
#pragma unroll
      for (int i = 0; i < 20; ++i) wm[i] = __builtin_amdgcn_raw_buffer_load_b128(rm, mo, i * 7680, 0);
#pragma unroll
      for (int u = 0; u < 3; ++u) {
        const float x = lo16(cv[u]);
        *(LAS float*)(smem + SIL + (G * 24 + (lane & 7) + 8 * u) * 4) = rbf(x * sigm(x));
      }
      f32x2 a[4];
#pragma unroll
      for (int e = 0; e < 4; ++e) a[e] = f32x2{0.f, 0.f};
#pragma unroll
      for (int i4 = 0; i4 < 5; ++i4) {
        const f32x4 s4 = LDSF4(SIL + (G * 24 + 4 * i4) * 4);
#pragma unroll
        for (int u = 0; u < 4; ++u) {
#pragma unroll
          for (int e = 0; e < 4; ++e) a[e] += f32x2{lo16(wm[4 * i4 + u][e]), hi16(wm[4 * i4 + u][e])} * s4[u];
        }
      }
      f32x2 y2;
      y2.x = swap16_add(swap32_add(a[0].x, a[2].x), swap32_add(a[1].x, a[3].x));
      y2.y = swap16_add(swap32_add(a[0].y, a[2].y), swap32_add(a[1].y, a[3].y));
      y2.x += dppf<0x128>(y2.x); y2.y += dppf<0x128>(y2.y);
      if ((lane & 8) == 0) *(LAS f32x2*)(smem + RR + wv * 256 + (lane & 7) * 32 + lg * 8) = y2;
      DRAIN();
      STAMP(6);
    } else {
      DRAIN();
      if (is_gu && wave > 4) {
        __builtin_amdgcn_s_sleep(DLY_W);
        DMA_GU();
      }
    }
    BAR();
    DRAIN();
    STAMP(7);
    if (is_md && wave >= 5) {
      if (wave == 5) {
        f32x2 s = (*(LAS f32x2*)(smem + RR + (lane & 31) * 8) + *(LAS f32x2*)(smem + RR + 256 + (lane & 31) * 8)) +
                  (*(LAS f32x2*)(smem + RR + 512 + (lane & 31) * 8) + *(LAS f32x2*)(smem + RR + 768 + (lane & 31) * 8));
        u32 p = pkbf(s.x, s.y);
        if ((md_id >= 10 && md_id < 20) || (md_id >= 40 && md_id < 50)) p = pkbf(1.f + lo16(p), 1.f + hi16(p));
        if (lane < 32) ST32(san(p), OFF_MOD + md_id * 128 + lane * 4);
      }
      if (is_gu) { DMA_GU(); }
      else if (is_dn) {
        u32 vo = ((lane >> 1) + 32 * (wave - 5)) * 1280 + dn_id * 32 + (lane & 1) * 16;
        for (int i = wave - 5; i < 54; i += 3, vo += 96 * 1280) dma16(wd, vo, (u32)(uintptr_t)smem + i * 1024);
      }
    }
    if (wave < 4) { LOADMOD(); LNSTATS(); y0 = y1 = Row5{SENT4, SENT}; PINMOD(); }
  }

  f32x2 x1[2][5], cb1[5], sb[5], gb[5];
  if (wave < 4) {
#define MODOK() (__all(umax2(umax2(umax2(umax5(mp[0]), umax5(mp[1])), umax2(umax5(mp[2]), umax5(mp[3]))), umax2(umax5(mp[4]), umax5(mp[5]))) != SENT))
    if (!MODOK()) {
      _Pragma("unroll 1") for (u32 spins = 0; spins < SPIN_CAP; ++spins) {
        CFENCE();
        LOADMOD();
        if (MODOK()) break;
        __builtin_amdgcn_s_sleep(8);
      }
    }
    CFENCE();
    f32x2 hg[2][5], kd[2][5], gw[5], e0 = {0.f, 0.f}, e1 = e0;
#pragma unroll
    for (int c = 0; c < 5; ++c) {
      const f32x2 ga = up2(r5(mp[2], c)), caga = up2(r5(mp[1], c)) * ga, saga = up2(r5(mp[0], c)) * ga, wf = up2(r5(wrp, c)), Df = up2(r5(Dp, c));
      sb[c] = up2(r5(mp[3], c)); cb1[c] = up2(r5(mp[4], c)); gb[c] = up2(r5(mp[5], c));
      gw[c] = cb1[c] * wf; e0 += gw[c]; e1 += sb[c] * wf;
#pragma unroll
      for (int t = 0; t < 2; ++t) {
        const f32x2 qf = up2(r5(qd[t], c));
        hg[t][c] = (qf * rs1[t] + nm1[t]) * caga + saga;
        kd[t][c] = (t ? f32x2{lo16(kp[c].x), lo16(kp[c].y)} : f32x2{hi16(kp[c].x), hi16(kp[c].y)}) * Df * hg[t][c] + qf;
      }
    }
    float ex = swap32_add(hsum(e0), hsum(e1));
#pragma unroll
    for (int c = 0; c < 5; ++c)
      asm volatile("" : "+v"(hg[0][c]), "+v"(hg[1][c]), "+v"(kd[0][c]), "+v"(kd[1][c]), "+v"(gw[c]), "+v"(cb1[c]), "+v"(sb[c]), "+v"(gb[c]), "+v"(ex));
    STAMP(10);
    if (!is_conv) {
      const u32 po = OFF_P + (j * 5 / 3) * 128;
      _Pragma("unroll 1") for (u32 spins = 0; spins < SPIN_CAP; ++spins) {
        CFENCE();
        const u32x2 f = LD64(po);
        if (TAGOK(f)) break;
        __builtin_amdgcn_s_sleep(4);
      }
      __builtin_amdgcn_s_sleep(DLY_Y);
    }
    if (!YOK()) {
      _Pragma("unroll 1") for (u32 spins = 0; spins < SPIN_CAP; ++spins) {
        CFENCE();
        LOADY();
        if (YOK()) break;
        __builtin_amdgcn_s_sleep(2);
      }
    }
    DRAIN();
    STAMP(11);
    quad_tr(y0.a, y1.a);
    float a[2][3];
#pragma unroll
    for (int t = 0; t < 2; ++t) {
#pragma unroll
      for (int c = 0; c < 5; ++c) x1[t][c] = hg[t][c] * (t ? f32x2{hi16(r5(y0, c)), hi16(r5(y1, c))} : f32x2{lo16(r5(y0, c)), lo16(r5(y1, c))}) + kd[t][c];
      a[t][0] = hsum(((x1[t][0] + x1[t][1]) + (x1[t][2] + x1[t][3])) + x1[t][4]);
      a[t][1] = hsum(x1[t][0] * x1[t][0] + (x1[t][1] * x1[t][1] + (x1[t][2] * x1[t][2] + (x1[t][3] * x1[t][3] + x1[t][4] * x1[t][4]))));
      a[t][2] = hsum(x1[t][0] * gw[0] + (x1[t][1] * gw[1] + (x1[t][2] * gw[2] + (x1[t][3] * gw[3] + x1[t][4] * gw[4]))));
    }
    float R1 = swap16_add(swap32_add(a[0][0], a[1][0]), swap32_add(a[0][1], a[1][1]));
    float R2 = swap16_add(swap32_add(a[0][2], a[1][2]), ex);
    asm volatile("s_nop 1\n\tv_add_f32_dpp %0, %0, %0 row_ror:4" DPP_ALL "v_add_f32_dpp %1, %1, %1 row_ror:4" DPP_ALL
                 "s_nop 0\n\tv_add_f32_dpp %0, %0, %0 row_ror:8" DPP_ALL "v_add_f32_dpp %1, %1, %1 row_ror:8" DPP_ALL : "+v"(R1), "+v"(R2));
    if ((lane & 12) == 0) {
      LAS float* o = (LAS float*)(smem + RED2 + wave * 128 + (2 * (lane & 3) + (lane >> 5)) * 16 + (lg & 1) * 4);
      o[0] = R1; o[2] = R2;
    }
  } else {
    DRAIN();
    __builtin_amdgcn_s_setprio(3);
  }
  BAR();
  STAMP(12);
  const __amdgpu_buffer_rsrc_t ro = mkrs(out, S_ * D_ * 2);
  u32 w0 = 0, w1 = 0;
  u32 ov = 0, gv = 0;
  if (wave < 4 || wave == 5) {
    const f32x4 s = (LDSF4(RED2 + tk * 16) + LDSF4(RED2 + 128 + tk * 16)) + (LDSF4(RED2 + 256 + tk * 16) + LDSF4(RED2 + 384 + tk * 16));
    const float Gs = rdl(s.w, 0), c0s = rdl(s.w, 4);
    const float m0 = s.x * (1.f / D_), m = rbf(m0);
    const float rstd = __builtin_amdgcn_rcpf(rbf(__builtin_amdgcn_sqrtf(rbf(rbf(fmaxf(s.y * (1.f / D_) - m0 * m0, 0.f)) + 1e-5f))));
    const float r = rstd * (s.z - m * Gs) + c0s;
    u32 ob = __float_as_uint(r);
    ob ^= (u32)((int)ob >> 31) | 0x80000000u;
    ob = ob < 0xFFFFE000u ? ob : 0xFFFFE000u;
    const u32 key = (ob & 0xFFFFF000u) | ((2047u - (8 * j + tk)) << 1);
    u32 mx = umax2(key, dppu<0x124>(key)); mx = umax2(mx, dppu<0x122>(mx)); mx = umax2(mx, dppu<0x121>(mx));
    u32 k2 = key == mx ? 0u : key;
    k2 = umax2(k2, dppu<0x124>(k2)); k2 = umax2(k2, dppu<0x122>(k2)); k2 = umax2(k2, dppu<0x121>(k2));
    const u32 kA = RFL(mx), kB = RFL(k2) | 1u;
    if (wave == 5) {
      if (lane == 0) ST64((u32x2{kA, kB}), OFF_SCORE + j * 16);
      if (is_conv && lane == 0) ST64((u32x2{tl, th}), OFF_P + grp * 128 + 64);
      STAMP(1);
      if (is_gu || is_dn) {
        _Pragma("unroll 1") for (u32 spins = 0; spins < SPIN_CAP; ++spins) {
          CFENCE();
          const u32x2 f = LD64(OFF_P + (j * 5 / 3) * 128 + 64);
          if (TAGOK(f)) break;
          __builtin_amdgcn_s_sleep(2);
        }
        __builtin_amdgcn_s_sleep(DLY_SC);
        if (DLY_SD && is_dn) __builtin_amdgcn_s_sleep(DLY_SD);
        u32x2 g[4];
        _Pragma("unroll 1") for (u32 spins = 0; spins < SPIN_CAP; ++spins) {
          CFENCE();
#pragma unroll
          for (int i = 0; i < 4; ++i) g[i] = LD64(OFF_SCORE + lane * 16 + i * 1024);
          if (__all(umax2(umax2(umax2(g[0].x, g[0].y), umax2(g[1].x, g[1].y)), umax2(umax2(g[2].x, g[2].y), umax2(g[3].x, g[3].y))) != SENT)) break;
          __builtin_amdgcn_s_sleep(2);
        }
        CFENCE();
        STAMP(13);
        w0 = RFL(wave_umax(umax2(umax2(g[0].x, g[1].x), umax2(g[2].x, g[3].x))));
        u32 c2 = 0;
#pragma unroll
        for (int i = 0; i < 4; ++i) c2 = umax2(c2, g[i].x == w0 ? g[i].y : g[i].x);
        w1 = RFL(wave_umax(c2));
        const u32 wk = lane < 32 ? w0 : w1;
        const u32 tok = 2047 - ((wk >> 1) & 2047);
        if (is_gu) {
          const u32 l5 = lane & 31, o2 = 1024 + (l5 & 15) * 16;
          const u32 row = OFF_CAND + (tok >> 3) * 2560 + (wk & 1) * 1280;
          u32x4 h0, h1, h2;
          _Pragma("unroll 1") for (u32 spins = 0; spins < SPIN_CAP; ++spins) {
            CFENCE();
            h0 = LD128(row + l5 * 16); h1 = LD128(row + 512 + l5 * 16); h2 = LD128(row + o2);
            if (__all(umax2(umax2(umax4(h0), umax4(h1)), umax4(h2)) != SENT)) break;
            __builtin_amdgcn_s_sleep(1);
          }
          CFENCE();
          const u32 hb = HR + (lane >> 5) * HR_STRIDE;
          LDS128(hb + l5 * 16) = h0; LDS128(hb + 512 + l5 * 16) = h1; LDS128(hb + o2) = h2;
        } else {
          if (dn_id == 0 && lane == 0) ST64((u32x2{tl, th}), OFF_E);
          _Pragma("unroll 1") for (u32 spins = 0; spins < SPIN_CAP; ++spins) {
            CFENCE();
            const u32x2 f = LD64(OFF_RD + (tok >> 3) * 64 + (dn_id / 10) * 8);
            if (TAGOK(f)) break;
            __builtin_amdgcn_s_sleep(1);
          }
          CFENCE();
          ov = __builtin_amdgcn_raw_buffer_load_b16(ro, tok * 1280 + dn_id * 32 + li * 2, 0, 16);
          gv = __builtin_amdgcn_raw_buffer_load_b16(rs, OFF_MOD + 5 * 1280 + dn_id * 32 + li * 2, 0, 16);
        }
      }
    } else {
      const int tA = (2047 - ((kA >> 1) & 2047)) & 7, tB = (2047 - ((kB >> 1) & 2047)) & 7;
      const float nmr = -m * rstd, r2 = dppf<0x124>(rstd), n2 = dppf<0x124>(nmr);
      const float rs_[2] = {odd4 ? r2 : rstd, odd4 ? rstd : r2}, nm_[2] = {odd4 ? n2 : nmr, odd4 ? nmr : n2};
      Row5 op[2];
#pragma unroll
      for (int t = 0; t < 2; ++t) {
        u32 hq[5], oq[5];
#pragma unroll
        for (int c = 0; c < 5; ++c) {
          const f32x2 h2 = (x1[t][c] * rs_[t] + nm_[t]) * cb1[c] + sb[c];
          const f32x2 o = gb[c] * h2 + x1[t][c];
          oq[c] = pkbf(o.x, o.y);
          hq[c] = san(pkbf(h2.x, h2.y));
        }
        op[t] = Row5{u32x4{oq[0], oq[1], oq[2], oq[3]}, oq[4]};
        const int tok = 2 * (lane & 3) + t;
        if (tok == tA || tok == tB) {
          st5<16>(Row5{u32x4{hq[0], hq[1], hq[2], hq[3]}, hq[4]}, rs, OFF_CAND + j * 2560 + (tok == tB ? 1280 : 0) + cho, 0);
          st5<16>(op[t], ro, cho + tok * 1280, j * 10240);
        }
      }
#pragma unroll
      for (int t = 0; t < 2; ++t) {
        const int tok = 2 * (lane & 3) + t;
        if (tok != tA && tok != tB) st5<0>(op[t], ro, cho + tok * 1280, j * 10240);
      }
      if (!is_gu && !is_dn) {
        DRAIN();
        if (lane == 0) ST64((u32x2{tl, th}), OFF_RD + j * 64 + wave * 8);
        if (WBL && slot == 19 && wave == 0) {
          __builtin_amdgcn_s_sleep(WBL);
          asm volatile("buffer_wbl2 sc1\n\ts_waitcnt vmcnt(0)" ::: "memory");
        }
      }
    }
  }
  if (is_gu) {
    u32x4 Wf[20];
    if (wave < 4) {
      LAS char* p = smem + ((li >> 1) & 1) * UPB + (8 * lg + (li >> 2)) * 64 + wave * 16 + (li & 1) * 8;
#pragma unroll
      for (int s = 0; s < 20; ++s) Wf[s] = tr_frag(p + s * 2048, 256);
    }
    if (wave < 4) {
      DRAIN();
      if (lane == 0) ST64((u32x2{tl, th}), OFF_RD + j * 64 + wave * 8);
    }
    BAR();
    STAMP(14);
    if (wave < 4) {
      f32x4 a0 = {0.f, 0.f, 0.f, 0.f}, a1 = a0;
      const u32 hb = HR + ((li >> 2) & 1) * HR_STRIDE + lg * 16;
#pragma unroll
      for (int s = 0; s < 20; s += 2) {
        a0 = mfma16(LDS128(hb + s * 64), Wf[s], a0);
        a1 = mfma16(LDS128(hb + s * 64 + 64), Wf[s + 1], a1);
      }
      const float t = rbf(a0.x + a1.x);
      const float av = rbf(t * sigm(t)) * dppf<0x128>(t);
      const u32 pk = san(pkbf(av, dppf<0xF5>(av)));
      if ((lane & 0x29) == 0) ST32(pk, OFF_A + gu_id * 128 + lg * 64 + wave * 16 + li * 2);
    }
    STAMP(15);
  } else if (is_dn) {
    u32x4 Wf[7];
    LAS char* p = smem + (32 * wave + 8 * lg + (li >> 2)) * 32 + (li & 3) * 8;
#pragma unroll
    for (int s = 0; s < 6; ++s) Wf[s] = tr_frag(p + s * 8192, 128);
    Wf[6] = tr_frag(p + (wave < 6 ? 6 * 8192 : 0), 128);
    if (wave < 4) {
      DRAIN();
      if (lane == 0) ST64((u32x2{tl, th}), OFF_RD + j * 64 + wave * 8);
    }
    BAR();
    __builtin_amdgcn_s_sleep(DLY_A - DLY_SD);
    const u32 wk = lane < 32 ? w0 : w1, kb = wk & 0xFFFFF000u;
    float sg = rbf(sigm(rbf(__uint_as_float((kb & 0x80000000u) ? (kb ^ 0x80000000u) : ~kb)))), of = lo16(ov), gf = lo16(gv);
    u32 oo = (2047 - ((wk >> 1) & 2047)) * 1280 + dn_id * 32 + li * 2;
    asm volatile("" : "+v"(sg), "+v"(of), "+v"(gf), "+v"(oo));
    u32x4 Bf[7];
    const u32 ao = OFF_A + wave * 128 + ((li >> 2) & 1) * 64 + lg * 16;
    _Pragma("unroll 1") for (u32 spins = 0; spins < SPIN_CAP; ++spins) {
      CFENCE();
      u32 mx = 0;
#pragma unroll
      for (int s = 0; s < 6; ++s) Bf[s] = LD128(ao + s * 1024);
      Bf[6] = LD128(ao + (wave < 6 ? 6 * 1024 : 0));
#pragma unroll
      for (int s = 0; s < 7; ++s) mx = umax2(mx, umax4(Bf[s]));
      if (__all(mx != SENT)) break;
      __builtin_amdgcn_s_sleep(1);
    }
    CFENCE();
    STAMP(14);
    f32x4 a0 = {0.f, 0.f, 0.f, 0.f}, a1 = a0;
#pragma unroll
    for (int s = 0; s < 6; s += 2) { a0 = mfma16(Bf[s], Wf[s], a0); a1 = mfma16(Bf[s + 1], Wf[s + 1], a1); }
    if (wave < 6) a0 = mfma16(Bf[6], Wf[6], a0);
    if (lane < 32) *(LAS float*)(smem + RR + lane * 32 + wave * 4) = a0.x + a1.x;
    BAR();
    if (wave == 5) {
      const u32 ro_ = RR + ((lane >> 5) * 16 + li) * 32;
      const f32x4 s4 = LDSF4(ro_) + LDSF4(ro_ + 16);
      const float o = of + gf * rbf(rbf((s4.x + s4.y) + (s4.z + s4.w)) * sg);
      if ((lane & 16) == 0) __builtin_amdgcn_raw_buffer_store_b16((u16)pkbf(o, o), ro, oo, 0, 16);
    }
    STAMP(15);
  }
  if (CPF) asm volatile(".Lcode_to:");
}

extern "C" void kernel_launch(void* const* d_in, const int* in_sizes, int n_in, void* d_out, int out_size,
                              void* d_ws, size_t ws_size, hipStream_t stream) {
  (void)in_sizes; (void)n_in; (void)out_size; (void)ws_size;
  (void)hipFuncSetAttribute((const void*)fused_block, hipFuncAttributeMaxDynamicSharedMemorySize, (int)LDS_BYTES);
  fused_block<<<256, 512, LDS_BYTES, stream>>>(
      (const u16*)d_in[0], (const u16*)d_in[1], (const u16*)d_in[2], (const u16*)d_in[3], (const u16*)d_in[4],
      (const u16*)d_in[5], (const u16*)d_in[7], (const u16*)d_in[8], (const u16*)d_in[9], (const u16*)d_in[10],
      (const u16*)d_in[11], (const u16*)d_in[12], (u16*)d_out, (char*)d_ws);
}
```
